```python
import torch

def _softmax(x, axis=-1):
    return torch.softmax(x, dim=axis)
def _gelu(x):
    return torch.nn.functional.gelu(x, approximate="tanh")
def _silu(x):
    return torch.nn.functional.silu(x)
def _sigmoid(x):
    return torch.sigmoid(x)
def _layernorm(x, axis=-1, eps=1e-5):
    m = x.mean(dim=axis, keepdim=True)
    v = x.var(dim=axis, keepdim=True, unbiased=False)
    return (x - m) / torch.sqrt(v + eps)
def _scatter_add(tgt, idx, src, axis=0):
    return tgt.clone().index_add_(axis, idx, src)
def _segment_sum(src, seg, n):
    out = torch.zeros((n,) + src.shape[1:], dtype=src.dtype)
    return out.index_add_(0, seg, src)
def _pad(x, pw):
    flat = tuple(v for pair in reversed(pw) for v in pair)
    return torch.nn.functional.pad(x, flat)

kernel_name = 'concat_heads__mha_block_cached__gqa_block_cached__near_ridge'

def setup_inputs():

    _w = (lambda a: (lambda t: t.to(torch.bfloat16) if t.is_floating_point() else t)(torch.as_tensor(a)))
    def _A_setup():
        B, Sq, D = 8, 144, 448
        H, Sk, Dh = 16, 512, 256
        return {
            "x": _w((lambda *s: torch.randn(*s, dtype=torch.bfloat16) * 0.8)(B, Sq, D)),
            "k_cache": _w((lambda *s: torch.randn(*s, dtype=torch.bfloat16) * 0.8)(B, H, Sk, Dh)),
            "v_cache": _w((lambda *s: torch.randn(*s, dtype=torch.bfloat16) * 0.05)(B, H, Sk, Dh)),
            "wq": _w((lambda *s: torch.randn(*s, dtype=torch.bfloat16) * 0.8)(D, H * Dh) * (D ** -0.5)),
            "wo": _w((lambda *s: torch.randn(*s, dtype=torch.bfloat16) * 0.05)(H * Dh, D) * ((H * Dh) ** -0.5)),
        }
    _A_in = _A_setup()

    def _B_setup():
        B, Sq, D = 8, 144, 448
        Hq, Hkv, Sk, Dh = 16, 16, 512, 256
        assert Hq % Hkv == 0, (Hq, Hkv)
        return {
            "x": _w((lambda *s: torch.randn(*s, dtype=torch.bfloat16) * 0.8)(B, Sq, D)),
            "k_cache": _w((lambda *s: torch.randn(*s, dtype=torch.bfloat16) * 0.8)(B, Hkv, Sk, Dh)),
            "v_cache": _w((lambda *s: torch.randn(*s, dtype=torch.bfloat16) * 0.05)(B, Hkv, Sk, Dh)),
            "wq": _w((lambda *s: torch.randn(*s, dtype=torch.bfloat16) * 0.8)(D, Hq * Dh) * (D ** -0.5)),
            "wo": _w((lambda *s: torch.randn(*s, dtype=torch.bfloat16) * 0.05)(Hq * Dh, D) * ((Hq * Dh) ** -0.5)),
        }
    _B_in = _B_setup()

    return {
        "A_x": _A_in["x"],
        "A_k_cache": _A_in["k_cache"],
        "A_v_cache": _A_in["v_cache"],
        "A_wq": _A_in["wq"],
        "A_wo": _A_in["wo"],
        "B_x": _B_in["x"],
        "B_k_cache": _B_in["k_cache"],
        "B_v_cache": _B_in["v_cache"],
        "B_wq": _B_in["wq"],
        "B_wo": _B_in["wo"],
    }

def reference(A_x, A_k_cache, A_v_cache, A_wq, A_wo, B_x, B_k_cache, B_v_cache, B_wq, B_wo):
    def _A_ref(x, k_cache, v_cache, wq, wo):
        B, Sq, D = x.shape
        H, Dh = 16, 256
        q = (lambda x, *d: x.permute(*d))((x @ wq).reshape(B, Sq, H, Dh), 0, 2, 1, 3)
        scores = torch.einsum("bhqd,bhkd->bhqk", q, k_cache) * (Dh ** -0.5)
        p = _softmax(scores, axis=-1)
        o = torch.einsum("bhqk,bhkd->bhqd", p, v_cache)
        return (lambda x, *d: x.permute(*d))(o, 0, 2, 1, 3).reshape(B, Sq, H * Dh) @ wo

    def _B_ref(x, k_cache, v_cache, wq, wo):
        B, Sq, D = x.shape
        Hq, Dh = 16, 256
        Hkv = k_cache.shape[1]
        rep = Hq // Hkv
        q = (lambda x, *d: x.permute(*d))((x @ wq).reshape(B, Sq, Hq, Dh), 0, 2, 1, 3)
        k = (lambda x, n, axis: torch.repeat_interleave(x, n, dim=axis))(k_cache, rep, axis=1)
        v = (lambda x, n, axis: torch.repeat_interleave(x, n, dim=axis))(v_cache, rep, axis=1)
        scores = torch.einsum("bhqd,bhkd->bhqk", q, k) * (Dh ** -0.5)
        p = _softmax(scores, axis=-1)
        o = torch.einsum("bhqk,bhkd->bhqd", p, v)
        return (lambda x, *d: x.permute(*d))(o, 0, 2, 1, 3).reshape(B, Sq, Hq * Dh) @ wo

    a = _A_ref(A_x, A_k_cache, A_v_cache, A_wq, A_wo)
    b = _B_ref(B_x, B_k_cache, B_v_cache, B_wq, B_wo)
    return torch.concatenate([a, b], axis=-1)
```

```cpp
#include <hip/hip_runtime.h>
#include <hip/hip_bf16.h>
#include <stdint.h>


namespace {

typedef __attribute__((ext_vector_type(8))) short bf16x8;
typedef __attribute__((ext_vector_type(4))) short s16x4;
typedef __attribute__((ext_vector_type(4))) float f32x4;
typedef __attribute__((ext_vector_type(4))) unsigned u32x4;
typedef __attribute__((ext_vector_type(2))) unsigned u32x2;
typedef __attribute__((address_space(3))) s16x4* lds_tr_ptr;

constexpr int NB = 8, SQ = 144, DM = 448, NH = 16, SK = 512, DH = 256;
constexpr int NT = 9;

constexpr int LDS_A = 0;
constexpr int LDS_B = 73728;
constexpr int LDS_STAT = 155648;
constexpr int LDS_TOTAL = 163840;
constexpr int QSTAGE = 51200;
constexpr int QX = 18432;

__device__ __forceinline__ void dma16(const void* sbase, unsigned voff, unsigned lds_dst) {
  unsigned keep;
  asm volatile("s_mov_b32 %0, m0\n\ts_mov_b32 m0, %3\n\ts_nop 0\n\tglobal_load_lds_dwordx4 %1, %2\n\ts_mov_b32 m0, %0"
               : "=&s"(keep) : "v"(voff), "s"(sbase), "s"(lds_dst) : "memory");
}
#define WAIT_VM(n) asm volatile("s_waitcnt vmcnt(" #n ")" ::: "memory")
#define WAIT_LGKM0() asm volatile("s_waitcnt lgkmcnt(0)" ::: "memory")
__device__ __forceinline__ void bar() { WAIT_LGKM0(); __builtin_amdgcn_s_barrier(); asm volatile("" ::: "memory"); }

typedef __attribute__((ext_vector_type(2))) __bf16 bf16x2_t;
typedef __attribute__((ext_vector_type(2))) float f32x2;
__device__ __forceinline__ unsigned cvtpk(float lo, float hi) {
  f32x2 v = {lo, hi};
  bf16x2_t r = __builtin_convertvector(v, bf16x2_t);
  return __builtin_bit_cast(unsigned, r);
}
__device__ __forceinline__ bf16x8 tr2(const char* smem, unsigned a0, unsigned a1) {
  s16x4 l = __builtin_amdgcn_ds_read_tr16_b64_v4i16((lds_tr_ptr)(smem + a0));
  s16x4 h = __builtin_amdgcn_ds_read_tr16_b64_v4i16((lds_tr_ptr)(smem + a1));
  return (bf16x8){l[0], l[1], l[2], l[3], h[0], h[1], h[2], h[3]};
}
#define MFMA(a, b, c) __builtin_amdgcn_mfma_f32_16x16x32_bf16(a, b, c, 0, 0, 0)

__global__ __launch_bounds__(512, 2) void fused_attn(
    const __hip_bfloat16* __restrict__ xA, const __hip_bfloat16* __restrict__ kA, const __hip_bfloat16* __restrict__ vA,
    const __hip_bfloat16* __restrict__ wqA, const __hip_bfloat16* __restrict__ woA,
    const __hip_bfloat16* __restrict__ xB, const __hip_bfloat16* __restrict__ kB, const __hip_bfloat16* __restrict__ vB,
    const __hip_bfloat16* __restrict__ wqB, const __hip_bfloat16* __restrict__ woB,
    float* __restrict__ partial) {
  extern __shared__ __attribute__((aligned(16))) char smem[];
  const int tid = threadIdx.x;
  const int lane = tid & 63;
  const int w = __builtin_amdgcn_readfirstlane(tid >> 6);
  const int l15 = lane & 15, g = lane >> 4, q4 = (lane >> 2) & 3, p4 = lane & 3, hi = lane >> 5;

  const int bid = blockIdx.x;
  const int xcd = bid & 7, rr = bid >> 3;
  const int p = xcd >> 2, h = (xcd & 3) * 4 + (rr >> 3), b = rr & 7;

  const char* xb = (const char*)((p ? xB : xA) + (size_t)b * SQ * DM);
  const char* kh = (const char*)((p ? kB : kA) + (size_t)(b * NH + h) * SK * DH);
  const char* vh = (const char*)((p ? vB : vA) + (size_t)(b * NH + h) * SK * DH);
  const char* wqh = (const char*)((p ? wqB : wqA) + h * DH);
  const char* woh = (const char*)((p ? woB : woA) + (size_t)h * DH * DM);

  const unsigned x_voff = (unsigned)((8 * w + (lane >> 3)) * 896 + (((lane & 7) ^ ((((w & 1) << 2) + g) & 7)) << 4));
  unsigned wq_voff[4];
#pragma unroll
  for (int jj = 0; jj < 4; ++jj) {
    const int r = 2 * jj + hi;
    const int f = (r & 3) | ((w & 1) << 2);
    wq_voff[jj] = (unsigned)((8 * w + r) * 8192 + (((lane & 31) ^ (f << 1)) << 4));
  }
  auto issue_qstage = [&](int s, int buf) {
    const char* xs = xb + s * 128;
    const unsigned dst = buf * QSTAGE;
    dma16(xs, x_voff, dst + w * 1024);
    dma16(xs + 64 * 896, x_voff, dst + (w + 8) * 1024);
    if (w < 2) dma16(xs + 128 * 896, x_voff, dst + (w + 16) * 1024);
    const char* ws = wqh + (size_t)s * 64 * 8192;
#pragma unroll
    for (int jj = 0; jj < 4; ++jj) dma16(ws, wq_voff[jj], dst + QX + (4 * w + jj) * 1024);
  };
  const int ftr = q4 | ((g & 1) << 2);
  const unsigned qa0 = QX + (8 * g + q4) * 512 + (((2 * w) ^ ftr) << 5) + p4 * 8;
  const unsigned qa1 = QX + (8 * g + q4) * 512 + (((2 * w + 1) ^ ftr) << 5) + p4 * 8;
  const unsigned xr0 = l15 * 128 + ((g ^ (l15 >> 1)) << 4);
  const unsigned xr1 = xr0 ^ 64;

  f32x4 qacc[2][NT];
#pragma unroll
  for (int m = 0; m < 2; ++m)
#pragma unroll
    for (int n = 0; n < NT; ++n) qacc[m][n] = (f32x4){0.f, 0.f, 0.f, 0.f};

  issue_qstage(0, 0);
  issue_qstage(1, 1);
  {
    int buf = 0;
    for (int s = 0; s < 7; ++s) {
      if (s < 6) { if (w < 2) WAIT_VM(7); else WAIT_VM(6); } else WAIT_VM(0);
      bar();
      if (s + 2 < 7) issue_qstage(s + 2, buf == 0 ? 2 : buf - 1);
      const char* sb = smem + buf * QSTAGE;
#pragma unroll
      for (int ks = 0; ks < 2; ++ks) {
        bf16x8 a0 = tr2(sb, qa0 + ks * 16384, qa0 + ks * 16384 + 2048);
        bf16x8 a1 = tr2(sb, qa1 + ks * 16384, qa1 + ks * 16384 + 2048);
#pragma unroll
        for (int n = 0; n < NT; ++n) {
          bf16x8 bx = *(const bf16x8*)(sb + (ks ? xr1 : xr0) + n * 2048);
          qacc[0][n] = MFMA(a0, bx, qacc[0][n]);
          qacc[1][n] = MFMA(a1, bx, qacc[1][n]);
        }
      }
      buf = buf == 2 ? 0 : buf + 1;
    }
  }
  bar();

  unsigned v_voff[2];
#pragma unroll
  for (int jj = 0; jj < 2; ++jj) {
    const int r = 4 * w + 2 * jj + hi;
    v_voff[jj] = (unsigned)(r * 512 + (((lane & 31) ^ ((r & 7) << 1)) << 4));
  }
  auto issue_v = [&](int t) {
    const char* src = vh + (size_t)(64 * (t & 7) + 32 * (t >> 3)) * 512;
    const unsigned dst = LDS_B + ((t + 4) % 5) * 16384 + w * 2048;
    dma16(src, v_voff[0], dst);
    dma16(src, v_voff[1], dst + 1024);
  };
  issue_v(0); issue_v(1); issue_v(2); issue_v(3);

#pragma unroll
  for (int m = 0; m < 2; ++m)
#pragma unroll
    for (int n = 0; n < NT; ++n) {
      u32x2 v = {cvtpk(qacc[m][n][0], qacc[m][n][1]), cvtpk(qacc[m][n][2], qacc[m][n][3])};
      const unsigned a = (16 * n + l15) * 512 + (((4 * w + 2 * m + (g >> 1)) ^ l15) << 4) + (g & 1) * 8;
      *(u32x2*)(smem + LDS_A + a) = v;
    }
  bar();

  f32x4 sacc[4][NT];
#pragma unroll
  for (int m = 0; m < 4; ++m)
#pragma unroll
    for (int n = 0; n < NT; ++n) sacc[m][n] = (f32x4){0.f, 0.f, 0.f, 0.f};
  {
    const char* kp = kh + (size_t)(64 * w + l15) * 512 + g * 16;
    const unsigned rowb = LDS_A + l15 * 512;
    bf16x8 kf[3][4];
#pragma unroll
    for (int ks = 0; ks < 3; ++ks)
#pragma unroll
      for (int m = 0; m < 4; ++m) kf[ks][m] = *(const bf16x8*)(kp + m * 16 * 512 + ks * 64);
#pragma unroll
    for (int ks = 0; ks < 8; ++ks) {
      const unsigned ca = rowb + (((4 * ks + g) ^ l15) << 4);
#pragma unroll
      for (int n = 0; n < NT; ++n) {
        bf16x8 bq = *(const bf16x8*)(smem + ca + n * 8192);
#pragma unroll
        for (int m = 0; m < 4; ++m) sacc[m][n] = MFMA(kf[ks % 3][m], bq, sacc[m][n]);
      }
      if (ks + 3 < 8) {
#pragma unroll
        for (int m = 0; m < 4; ++m) kf[ks % 3][m] = *(const bf16x8*)(kp + m * 16 * 512 + (ks + 3) * 64);
      }
      __builtin_amdgcn_sched_barrier(0);
    }
  }

  float* smax = (float*)(smem + LDS_B + 3 * 16384);
  float* ssum = (float*)(smem + LDS_STAT);
#pragma unroll
  for (int n = 0; n < NT; ++n) {
    float m = sacc[0][n][0];
#pragma unroll
    for (int mm = 0; mm < 4; ++mm)
#pragma unroll
      for (int r = 0; r < 4; ++r) m = fmaxf(m, sacc[mm][n][r]);
    m = fmaxf(m, __shfl_xor(m, 16));
    m = fmaxf(m, __shfl_xor(m, 32));
    if (g == 0) smax[(16 * n + l15) * 8 + w] = m;
  }
  bar();
  constexpr float CEXP = 0.0625f * 1.4426950408889634f;
  u32x4 pk[2][NT];
#pragma unroll
  for (int n = 0; n < NT; ++n) {
    const f32x4 m0 = *(const f32x4*)(smax + (16 * n + l15) * 8);
    const f32x4 m1 = *(const f32x4*)(smax + (16 * n + l15) * 8 + 4);
    const float m = fmaxf(fmaxf(fmaxf(m0[0], m0[1]), fmaxf(m0[2], m0[3])), fmaxf(fmaxf(m1[0], m1[1]), fmaxf(m1[2], m1[3])));
    const float mc = -m * CEXP;
    float sum = 0.f;
#pragma unroll
    for (int mm = 0; mm < 4; ++mm)
#pragma unroll
      for (int r = 0; r < 4; ++r) {
        const float e = __builtin_amdgcn_exp2f(fmaf(sacc[mm][n][r], CEXP, mc));
        sacc[mm][n][r] = e; sum += e;
      }
    sum += __shfl_xor(sum, 16);
    sum += __shfl_xor(sum, 32);
    if (g == 0) ssum[(16 * n + l15) * 8 + w] = sum;
#pragma unroll
    for (int j = 0; j < 2; ++j)
      pk[j][n] = (u32x4){cvtpk(sacc[2 * j][n][0], sacc[2 * j][n][1]), cvtpk(sacc[2 * j][n][2], sacc[2 * j][n][3]),
                         cvtpk(sacc[2 * j + 1][n][0], sacc[2 * j + 1][n][1]), cvtpk(sacc[2 * j + 1][n][2], sacc[2 * j + 1][n][3])};
  }
  const unsigned pw = LDS_A + l15 * 512 + (((4 * w + g) ^ l15) << 4);
#pragma unroll
  for (int n = 0; n < NT; ++n) *(u32x4*)(smem + pw + n * 8192) = pk[0][n];

  f32x4 oacc[2][NT];
#pragma unroll
  for (int m = 0; m < 2; ++m)
#pragma unroll
    for (int n = 0; n < NT; ++n) oacc[m][n] = (f32x4){0.f, 0.f, 0.f, 0.f};
  const unsigned va0 = (4 * g + q4) * 512 + (((2 * w) ^ ftr) << 5) + p4 * 8;
  const unsigned va1 = (4 * g + q4) * 512 + (((2 * w + 1) ^ ftr) << 5) + p4 * 8;
  {
    const unsigned rowb = LDS_A + l15 * 512;
#pragma unroll 1
    for (int hf = 0; hf < 2; ++hf) {
#pragma unroll
      for (int t8 = 0; t8 < 8; ++t8) {
        const int t = hf * 8 + t8;
        if (t < 13) WAIT_VM(6); else if (t == 13) WAIT_VM(4); else if (t == 14) WAIT_VM(2); else WAIT_VM(0);
        bar();
        if (t + 4 < 16) issue_v(t + 4);
        const char* vs = smem + LDS_B + ((t + 4) % 5) * 16384;
        bf16x8 a0 = tr2(vs, va0, va0 + 8192);
        bf16x8 a1 = tr2(vs, va1, va1 + 8192);
        const unsigned ca = rowb + (((4 * t8 + g) ^ l15) << 4);
#pragma unroll
        for (int n = 0; n < NT; ++n) {
          bf16x8 bp = *(const bf16x8*)(smem + ca + n * 8192);
          oacc[0][n] = MFMA(a0, bp, oacc[0][n]);
          oacc[1][n] = MFMA(a1, bp, oacc[1][n]);
        }
      }
      if (hf == 0) {
        bar();
#pragma unroll
        for (int n = 0; n < NT; ++n) *(u32x4*)(smem + pw + n * 8192) = pk[1][n];
      }
    }
  }
  float rinv[NT];
#pragma unroll
  for (int n = 0; n < NT; ++n) {
    const f32x4 s0 = *(const f32x4*)(ssum + (16 * n + l15) * 8);
    const f32x4 s1 = *(const f32x4*)(ssum + (16 * n + l15) * 8 + 4);
    rinv[n] = 1.0f / (((s0[0] + s0[1]) + (s0[2] + s0[3])) + ((s1[0] + s1[1]) + (s1[2] + s1[3])));
  }
  bar();

  unsigned wo_voff[4];
#pragma unroll
  for (int i = 0; i < 4; ++i) {
    const int ci = 64 * (w + 8 * i) + lane;
    const int r = ci / 56, c = ci % 56;
    wo_voff[i] = (unsigned)(r * 896 + ((c ^ (((r >> 1) & 3) << 1)) << 4));
  }
  auto issue_wo = [&](int ks) {
    const char* src = woh + (size_t)ks * 32 * 896;
    const unsigned dst = LDS_B + (ks % 3) * 28672 + w * 1024;
    dma16(src, wo_voff[0], dst);
    dma16(src, wo_voff[1], dst + 8192);
    dma16(src, wo_voff[2], dst + 16384);
    if (w < 4) dma16(src, wo_voff[3], dst + 24576);
  };
  issue_wo(0); issue_wo(1);
#pragma unroll
  for (int n = 0; n < NT; ++n) {
    const float s = rinv[n];
    u32x4 v = {cvtpk(oacc[0][n][0] * s, oacc[0][n][1] * s), cvtpk(oacc[0][n][2] * s, oacc[0][n][3] * s),
               cvtpk(oacc[1][n][0] * s, oacc[1][n][1] * s), cvtpk(oacc[1][n][2] * s, oacc[1][n][3] * s)};
    *(u32x4*)(smem + pw + n * 8192) = v;
  }
  const int rh = w >> 2, cc = w & 3;
  const int swo = ((g & 1) << 1) | (q4 >> 1);
  unsigned oa[7];
#pragma unroll
  for (int i = 0; i < 7; ++i) oa[i] = (4 * g + q4) * 896 + (((7 * cc + i) ^ swo) << 5) + p4 * 8;
  f32x4 facc[7][5];
#pragma unroll
  for (int i = 0; i < 7; ++i)
#pragma unroll
    for (int n = 0; n < 5; ++n) facc[i][n] = (f32x4){0.f, 0.f, 0.f, 0.f};
  {
    const unsigned rowb = LDS_A + (rh * 80 + l15) * 512;
#pragma unroll 1
    for (int ks = 0; ks < 8; ++ks) {
      if (ks < 7) { if (w < 4) WAIT_VM(4); else WAIT_VM(3); } else WAIT_VM(0);
      bar();
      if (ks + 2 < 8) issue_wo(ks + 2);
      const char* wsb = smem + LDS_B + (ks % 3) * 28672;
      const unsigned ca = rowb + (((4 * ks + g) ^ l15) << 4);
      bf16x8 bo[5];
#pragma unroll
      for (int n = 0; n < 4; ++n) bo[n] = *(const bf16x8*)(smem + ca + n * 8192);
      if (rh == 0) bo[4] = *(const bf16x8*)(smem + ca + 4 * 8192);
#pragma unroll
      for (int i = 0; i < 7; ++i) {
        bf16x8 a = tr2(wsb, oa[i], oa[i] + 14336);
#pragma unroll
        for (int n = 0; n < 4; ++n) facc[i][n] = MFMA(a, bo[n], facc[i][n]);
        if (rh == 0) facc[i][4] = MFMA(a, bo[4], facc[i][4]);
      }
    }
  }
  float* pp = partial + (size_t)((p * NB + b) * NH + h) * (252 * 256) + lane * 4;
#pragma unroll
  for (int i = 0; i < 7; ++i) {
#pragma unroll
    for (int n = 0; n < 4; ++n) *(f32x4*)(pp + ((rh * 5 + n) * 28 + 7 * cc + i) * 256) = facc[i][n];
    if (rh == 0) *(f32x4*)(pp + (4 * 28 + 7 * cc + i) * 256) = facc[i][4];
  }
}

__global__ __launch_bounds__(256) void reduce_heads(const float* __restrict__ partial, __hip_bfloat16* __restrict__ out) {
  const int idx = blockIdx.x * 256 + threadIdx.x;
  const int lane = idx & 63;
  const int T = (idx >> 6) % 252;
  const int pb = (idx >> 6) / 252;
  if (pb >= 16) return;
  const float* src = partial + (size_t)pb * NH * (252 * 256) + T * 256 + lane * 4;
  f32x4 s = {0.f, 0.f, 0.f, 0.f};
#pragma unroll
  for (int hh = 0; hh < NH; ++hh) s += *(const f32x4*)(src + (size_t)hh * (252 * 256));
  const int n = T / 28, mt = T % 28;
  const int p = pb >> 3, b = pb & 7;
  const int row = 16 * n + (lane & 15), col = 16 * mt + 4 * (lane >> 4);
  u32x2 v = {cvtpk(s[0], s[1]), cvtpk(s[2], s[3])};
  *(u32x2*)((char*)out + ((size_t)(b * SQ + row) * (2 * DM) + p * DM + col) * 2) = v;
}

}

extern "C" void kernel_launch(void* const* d_in, const int* in_sizes, int n_in,
                              void* d_out, int out_size, void* d_ws, size_t ws_size,
                              hipStream_t stream) {
  (void)in_sizes; (void)n_in; (void)out_size; (void)ws_size;
  hipFuncSetAttribute((const void*)fused_attn, hipFuncAttributeMaxDynamicSharedMemorySize, LDS_TOTAL);
  float* partial = (float*)d_ws;
  fused_attn<<<256, 512, LDS_TOTAL, stream>>>(
      (const __hip_bfloat16*)d_in[0], (const __hip_bfloat16*)d_in[1], (const __hip_bfloat16*)d_in[2],
      (const __hip_bfloat16*)d_in[3], (const __hip_bfloat16*)d_in[4],
      (const __hip_bfloat16*)d_in[5], (const __hip_bfloat16*)d_in[6], (const __hip_bfloat16*)d_in[7],
      (const __hip_bfloat16*)d_in[8], (const __hip_bfloat16*)d_in[9], partial);
  reduce_heads<<<(16 * 252 * 64 + 255) / 256, 256, 0, stream>>>(partial, (__hip_bfloat16*)d_out);
}
```

```cpp
#include <hip/hip_runtime.h>
#include <hip/hip_bf16.h>
#include <stdint.h>


namespace {

typedef __attribute__((ext_vector_type(8))) short bf16x8;
typedef __attribute__((ext_vector_type(4))) short s16x4;
typedef __attribute__((ext_vector_type(4))) float f32x4;
typedef __attribute__((ext_vector_type(2))) float f32x2;
typedef __attribute__((ext_vector_type(4))) unsigned u32x4;
typedef __attribute__((ext_vector_type(2))) unsigned u32x2;
typedef __attribute__((ext_vector_type(2))) __bf16 bf16x2_t;
typedef __attribute__((address_space(3))) s16x4* lds_tr_ptr;

constexpr int NB = 8, SQ = 144, DM = 448, NH = 16, SK = 512, DH = 256;
constexpr int NT = 9;

constexpr int LDS_A = 0;
constexpr int LDS_B = 73728;
constexpr int LDS_STAT = 155648;
constexpr int LDS_TOTAL = 163840;
constexpr int QSTAGE = 51200;
constexpr int QX = 18432;

__device__ __forceinline__ void dma16(const void* sbase, unsigned voff, unsigned lds_dst) {
  unsigned keep;
  asm volatile("s_mov_b32 %0, m0\n\ts_mov_b32 m0, %3\n\ts_nop 0\n\tglobal_load_lds_dwordx4 %1, %2\n\ts_mov_b32 m0, %0"
               : "=&s"(keep) : "v"(voff), "s"(sbase), "s"(lds_dst) : "memory");
}
template <int N> __device__ __forceinline__ void wait_vm() { asm volatile("s_waitcnt vmcnt(%0)" ::"i"(N) : "memory"); }
#define WAIT_LGKM0() asm volatile("s_waitcnt lgkmcnt(0)" ::: "memory")
#define SB() __builtin_amdgcn_sched_barrier(0)
__device__ __forceinline__ void bar() { WAIT_LGKM0(); __builtin_amdgcn_s_barrier(); asm volatile("" ::: "memory"); }

__device__ __forceinline__ unsigned cvtpk(float lo, float hi) {
  f32x2 v = {lo, hi};
  bf16x2_t r = __builtin_convertvector(v, bf16x2_t);
  return __builtin_bit_cast(unsigned, r);
}
__device__ __forceinline__ bf16x8 tr2(const char* base, unsigned a0, unsigned a1) {
  s16x4 l = __builtin_amdgcn_ds_read_tr16_b64_v4i16((lds_tr_ptr)(base + a0));
  s16x4 h = __builtin_amdgcn_ds_read_tr16_b64_v4i16((lds_tr_ptr)(base + a1));
  return (bf16x8){l[0], l[1], l[2], l[3], h[0], h[1], h[2], h[3]};
}
__device__ __forceinline__ float xmax(float v) {
  auto a = __builtin_amdgcn_permlane16_swap(__float_as_uint(v), __float_as_uint(v), false, false);
  v = fmaxf(__uint_as_float(a[0]), __uint_as_float(a[1]));
  auto b = __builtin_amdgcn_permlane32_swap(__float_as_uint(v), __float_as_uint(v), false, false);
  return fmaxf(__uint_as_float(b[0]), __uint_as_float(b[1]));
}
__device__ __forceinline__ float xsum(float v) {
  auto a = __builtin_amdgcn_permlane16_swap(__float_as_uint(v), __float_as_uint(v), false, false);
  v = __uint_as_float(a[0]) + __uint_as_float(a[1]);
  auto b = __builtin_amdgcn_permlane32_swap(__float_as_uint(v), __float_as_uint(v), false, false);
  return __uint_as_float(b[0]) + __uint_as_float(b[1]);
}
template <int NM, int NR> __device__ __forceinline__ void ilv() {
#pragma unroll
  for (int i = 0; i < NM; ++i) {
    __builtin_amdgcn_sched_group_barrier(0x008, 1, 0);
    if (i < NR) __builtin_amdgcn_sched_group_barrier(0x100, 1, 0);
  }
}
#define MFMA(a, b, c) __builtin_amdgcn_mfma_f32_16x16x32_bf16(a, b, c, 0, 0, 0)

template <int NA, int NN> struct Frag { bf16x8 a[NA]; bf16x8 b[NN]; };
template <int NA, int NN>
__device__ __forceinline__ void mma(f32x4 (&acc)[NA][NN], const Frag<NA, NN>& f) {
#pragma unroll
  for (int n = 0; n < NN; ++n)
#pragma unroll
    for (int m = 0; m < NA; ++m) acc[m][n] = MFMA(f.a[m], f.b[n], acc[m][n]);
}

#define RELANE() int lane = threadIdx.x & 63; asm volatile("" : "+v"(lane)); \
  const int l15 = lane & 15, g = lane >> 4, q4 = (lane >> 2) & 3, p4 = lane & 3; (void)l15; (void)g; (void)q4; (void)p4;
struct Ctx {
  char* smem;
  int lane, w, l15, g, q4, p4, hi;
  const char *xb, *kh, *vh, *wqh, *woh;
};

template <int NN, int XCNT>
__device__ __forceinline__ void phase_q(const Ctx& c, int nb) {
  char* smem = c.smem;
  const int w = c.w, lane = c.lane, g = c.g, l15 = c.l15, q4 = c.q4, p4 = c.p4, hi = c.hi, cc = c.w & 3;
  const unsigned x_voff = (unsigned)((8 * w + (lane >> 3)) * 896 + (((lane & 7) ^ ((((w & 1) << 2) + g) & 7)) << 4));
  unsigned wq_voff[4];
#pragma unroll
  for (int jj = 0; jj < 4; ++jj) {
    const int r = 2 * jj + hi;
    const int f = (r & 3) | ((w & 1) << 2);
    wq_voff[jj] = (unsigned)((8 * w + r) * 8192 + (((lane & 31) ^ (f << 1)) << 4));
  }
  auto issue = [&](int s, int buf) {
    const char* xs = c.xb + s * 128;
    const unsigned dst = buf * QSTAGE;
    dma16(xs, x_voff, dst + w * 1024);
    dma16(xs + 64 * 896, x_voff, dst + (w + 8) * 1024);
    if (XCNT == 3) dma16(xs + 128 * 896, x_voff, dst + (w + 16) * 1024);
    const char* ws = c.wqh + (size_t)s * 64 * 8192;
#pragma unroll
    for (int jj = 0; jj < 4; ++jj) dma16(ws, wq_voff[jj], dst + QX + (4 * w + jj) * 1024);
  };
  constexpr int CNT = XCNT + 4;
  const int ftr = q4 | ((g & 1) << 2);
  unsigned qa[4];
#pragma unroll
  for (int m = 0; m < 4; ++m) qa[m] = QX + (8 * g + q4) * 512 + (((4 * cc + m) ^ ftr) << 5) + p4 * 8;
  const unsigned xr0 = (nb * 16 + l15) * 128 + ((g ^ (l15 >> 1)) << 4);
  const unsigned xr1 = xr0 ^ 64;
  auto load = [&](Frag<4, NN>& f, int buf, int ks) {
    const char* sb = smem + buf * QSTAGE;
#pragma unroll
    for (int m = 0; m < 4; ++m) f.a[m] = tr2(sb, qa[m] + ks * 16384, qa[m] + ks * 16384 + 2048);
#pragma unroll
    for (int n = 0; n < NN; ++n) f.b[n] = *(const bf16x8*)(sb + (ks ? xr1 : xr0) + n * 2048);
  };
  f32x4 acc[4][NN];
#pragma unroll
  for (int m = 0; m < 4; ++m)
#pragma unroll
    for (int n = 0; n < NN; ++n) acc[m][n] = (f32x4){0.f, 0.f, 0.f, 0.f};
  Frag<4, NN> f0, f1;
  issue(0, 0); issue(1, 1); issue(2, 2);
  wait_vm<2 * CNT>();
  bar();
  load(f0, 0, 0);
#pragma unroll
  for (int s = 0; s < 7; ++s) {
    const int buf = s % 3, nbuf = (s + 1) % 3;
    load(f1, buf, 1);
    mma<4, NN>(acc, f0);
    ilv<4 * NN, 8 + NN>();
    SB();
    if (s < 6) {
      if (s < 5) wait_vm<CNT>(); else wait_vm<0>();
      bar();
      if (s + 3 < 7) issue(s + 3, buf);
      load(f0, nbuf, 0);
    }
    mma<4, NN>(acc, f1);
    if (s < 6) ilv<4 * NN, 8 + NN>();
    SB();
  }
  bar();
#pragma unroll
  for (int m = 0; m < 4; ++m)
#pragma unroll
    for (int n = 0; n < NN; ++n) {
      u32x2 v = {cvtpk(acc[m][n][0], acc[m][n][1]), cvtpk(acc[m][n][2], acc[m][n][3])};
      const unsigned a = (16 * (nb + n) + l15) * 512 + (((8 * cc + 2 * m + (g >> 1)) ^ l15) << 4) + (g & 1) * 8;
      *(u32x2*)(smem + LDS_A + a) = v;
    }
}

__global__ __launch_bounds__(512, 2) void fused_attn(
    const __hip_bfloat16* __restrict__ xA, const __hip_bfloat16* __restrict__ kA, const __hip_bfloat16* __restrict__ vA,
    const __hip_bfloat16* __restrict__ wqA, const __hip_bfloat16* __restrict__ woA,
    const __hip_bfloat16* __restrict__ xB, const __hip_bfloat16* __restrict__ kB, const __hip_bfloat16* __restrict__ vB,
    const __hip_bfloat16* __restrict__ wqB, const __hip_bfloat16* __restrict__ woB,
    float* __restrict__ partial) {
  extern __shared__ __attribute__((aligned(16))) char smem[];
  const int tid = threadIdx.x;
  Ctx c;
  c.smem = smem;
  c.lane = tid & 63;
  c.w = __builtin_amdgcn_readfirstlane(tid >> 6);
  c.l15 = c.lane & 15; c.g = c.lane >> 4; c.q4 = (c.lane >> 2) & 3; c.p4 = c.lane & 3; c.hi = c.lane >> 5;
  const int w = c.w;

  const int bid = blockIdx.x;
  const int xcd = bid & 7, rr = bid >> 3;
  const int p = xcd >> 2, h = (xcd & 3) * 4 + (rr >> 3), b = rr & 7;

  c.xb = (const char*)((p ? xB : xA) + (size_t)b * SQ * DM);
  c.kh = (const char*)((p ? kB : kA) + (size_t)(b * NH + h) * SK * DH);
  c.vh = (const char*)((p ? vB : vA) + (size_t)(b * NH + h) * SK * DH);
  c.wqh = (const char*)((p ? wqB : wqA) + h * DH);
  c.woh = (const char*)((p ? woB : woA) + (size_t)h * DH * DM);

  if (w < 2) phase_q<5, 3>(c, 0); else if (w < 4) phase_q<5, 2>(c, 0); else phase_q<4, 2>(c, 5);
  unsigned v_voff;
  { RELANE(); v_voff = (unsigned)((lane >> 2) * 512 + 64 * w + (((lane & 3) ^ ((g & 1) << 1)) << 4)); }
  const unsigned vring = LDS_B + w * 10240;
  auto issue_v = [&](int t) {
    const char* src = c.vh + (size_t)(64 * (t & 7) + 32 * (t >> 3)) * 512;
    dma16(src, v_voff, vring + (t % 5) * 2048);
    dma16(src + 16 * 512, v_voff, vring + (t % 5) * 2048 + 1024);
  };
  issue_v(0); issue_v(1); issue_v(2); issue_v(3); issue_v(4);
  bar();
  f32x4 sacc[4][NT];
#pragma unroll
  for (int m = 0; m < 4; ++m)
#pragma unroll
    for (int n = 0; n < NT; ++n) sacc[m][n] = (f32x4){0.f, 0.f, 0.f, 0.f};
  {
    RELANE();
    const char* kp = c.kh + (size_t)(64 * w + l15) * 512 + g * 16;
    const unsigned rowb = LDS_A + l15 * 512;
    bf16x8 kf[3][4];
#pragma unroll
    for (int ks = 0; ks < 3; ++ks)
#pragma unroll
      for (int m = 0; m < 4; ++m) kf[ks][m] = *(const bf16x8*)(kp + m * 16 * 512 + ks * 64);
    bf16x8 bq[2][3];
    auto loadq = [&](bf16x8 (&d)[3], int idx) {
      const int ks = idx / 3, grp = idx % 3;
      const unsigned ca = rowb + (((4 * ks + g) ^ l15) << 4);
#pragma unroll
      for (int j = 0; j < 3; ++j) d[j] = *(const bf16x8*)(smem + ca + (grp * 3 + j) * 8192);
    };
    loadq(bq[0], 0);
#pragma unroll
    for (int idx = 0; idx < 24; ++idx) {
      const int ks = idx / 3, grp = idx % 3;
      if (idx + 1 < 24) loadq(bq[(idx + 1) & 1], idx + 1);
      SB();
#pragma unroll
      for (int j = 0; j < 3; ++j)
#pragma unroll
        for (int m = 0; m < 4; ++m) sacc[m][grp * 3 + j] = MFMA(kf[ks % 3][m], bq[idx & 1][j], sacc[m][grp * 3 + j]);
      if (grp == 2 && ks + 3 < 8) {
#pragma unroll
        for (int m = 0; m < 4; ++m) kf[ks % 3][m] = *(const bf16x8*)(kp + m * 16 * 512 + (ks + 3) * 64);
      }
      SB();
    }
  }
  float* smax = (float*)(smem + LDS_STAT);
  float* ssum = (float*)(smem + LDS_STAT);
  u32x4 pk1[NT];
  {
  RELANE();
#pragma unroll
  for (int n = 0; n < NT; ++n) {
    float m0 = fmaxf(fmaxf(sacc[0][n][0], sacc[0][n][1]), fmaxf(sacc[0][n][2], sacc[0][n][3]));
    float m1 = fmaxf(fmaxf(sacc[1][n][0], sacc[1][n][1]), fmaxf(sacc[1][n][2], sacc[1][n][3]));
    float m2 = fmaxf(fmaxf(sacc[2][n][0], sacc[2][n][1]), fmaxf(sacc[2][n][2], sacc[2][n][3]));
    float m3 = fmaxf(fmaxf(sacc[3][n][0], sacc[3][n][1]), fmaxf(sacc[3][n][2], sacc[3][n][3]));
    const float m = xmax(fmaxf(fmaxf(m0, m1), fmaxf(m2, m3)));
    smax[(16 * n + l15) * 8 + w] = m;
  }
  bar();
  constexpr float CEXP = 0.0625f * 1.4426950408889634f;
  float mc[NT];
#pragma unroll
  for (int n = 0; n < NT; ++n) {
    const f32x4 m0 = *(const f32x4*)(smax + (16 * n + l15) * 8);
    const f32x4 m1 = *(const f32x4*)(smax + (16 * n + l15) * 8 + 4);
    mc[n] = -CEXP * fmaxf(fmaxf(fmaxf(m0[0], m0[1]), fmaxf(m0[2], m0[3])), fmaxf(fmaxf(m1[0], m1[1]), fmaxf(m1[2], m1[3])));
  }
  float rsum[NT];
  const unsigned pw = LDS_A + l15 * 512 + (((4 * w + g) ^ l15) << 4);
#pragma unroll
  for (int n = 0; n < NT; ++n) {
    float e[4][4];
#pragma unroll
    for (int mm = 0; mm < 4; ++mm)
#pragma unroll
      for (int r = 0; r < 4; ++r) e[mm][r] = __builtin_amdgcn_exp2f(fmaf(sacc[mm][n][r], CEXP, mc[n]));
    const float s0 = (e[0][0] + e[0][1]) + (e[0][2] + e[0][3]), s1 = (e[1][0] + e[1][1]) + (e[1][2] + e[1][3]);
    const float s2 = (e[2][0] + e[2][1]) + (e[2][2] + e[2][3]), s3 = (e[3][0] + e[3][1]) + (e[3][2] + e[3][3]);
    rsum[n] = xsum((s0 + s1) + (s2 + s3));
    u32x4 p0 = {cvtpk(e[0][0], e[0][1]), cvtpk(e[0][2], e[0][3]), cvtpk(e[1][0], e[1][1]), cvtpk(e[1][2], e[1][3])};
    *(u32x4*)(smem + pw + n * 8192) = p0;
    pk1[n] = (u32x4){cvtpk(e[2][0], e[2][1]), cvtpk(e[2][2], e[2][3]), cvtpk(e[3][0], e[3][1]), cvtpk(e[3][2], e[3][3])};
  }
  bar();
#pragma unroll
  for (int n = 0; n < NT; ++n) ssum[(16 * n + l15) * 8 + w] = rsum[n];
  }
  f32x4 oacc[2][NT];
#pragma unroll
  for (int m = 0; m < 2; ++m)
#pragma unroll
    for (int n = 0; n < NT; ++n) oacc[m][n] = (f32x4){0.f, 0.f, 0.f, 0.f};
  {
    RELANE();
    const unsigned pw = LDS_A + l15 * 512 + (((4 * w + g) ^ l15) << 4);
    const unsigned va0 = vring + (4 * g + q4) * 64 + ((0 ^ (g & 1)) << 5) + p4 * 8;
    const unsigned va1 = vring + (4 * g + q4) * 64 + ((1 ^ (g & 1)) << 5) + p4 * 8;
    const unsigned rowb = LDS_A + l15 * 512;
    auto load = [&](Frag<2, NT>& f, int t) {
      const char* vs = smem + (t % 5) * 2048;
      f.a[0] = tr2(vs, va0, va0 + 1024);
      f.a[1] = tr2(vs, va1, va1 + 1024);
      const unsigned ca = rowb + (((4 * (t & 7) + g) ^ l15) << 4);
#pragma unroll
      for (int n = 0; n < NT; ++n) f.b[n] = *(const bf16x8*)(smem + ca + n * 8192);
    };
    Frag<2, NT> f0, f1;
    wait_vm<8>();
    load(f0, 0);
    auto step = [&](Frag<2, NT>& cur, Frag<2, NT>& nxt, int t) {
      if (t < 15) {
        WAIT_LGKM0();
        if (t + 5 < 16) issue_v(t + 5);
        if (t + 5 < 16) wait_vm<8>(); else if (t == 11) wait_vm<6>(); else if (t == 12) wait_vm<4>(); else if (t == 13) wait_vm<2>(); else wait_vm<0>();
        if (t == 7) {
          bar();
#pragma unroll
          for (int n = 0; n < NT; ++n) *(u32x4*)(smem + pw + n * 8192) = pk1[n];
          bar();
        }
        load(nxt, t + 1);
      }
      mma<2, NT>(oacc, cur);
      if (t < 15) ilv<18, 13>();
      SB();
    };
#pragma unroll
    for (int t = 0; t < 16; t += 2) { step(f0, f1, t); step(f1, f0, t + 1); }
  }
  RELANE();
  const unsigned pw = LDS_A + l15 * 512 + (((4 * w + g) ^ l15) << 4);
  float rinv[NT];
#pragma unroll
  for (int n = 0; n < NT; ++n) {
    const f32x4 s0 = *(const f32x4*)(ssum + (16 * n + l15) * 8);
    const f32x4 s1 = *(const f32x4*)(ssum + (16 * n + l15) * 8 + 4);
    rinv[n] = 1.0f / (((s0[0] + s0[1]) + (s0[2] + s0[3])) + ((s1[0] + s1[1]) + (s1[2] + s1[3])));
  }
  bar();

  const unsigned wo_voff = (unsigned)((lane >> 3) * 896 + 128 * w + (((lane & 7) ^ (g << 1)) << 4));
  const unsigned woring = LDS_B + w * 12288;
  auto issue_wo = [&](int ks) {
    const char* src = c.woh + (size_t)ks * 32 * 896;
    const unsigned dst = woring + (ks % 3) * 4096;
#pragma unroll
    for (int j = 0; j < 4; ++j) dma16(src + j * 8 * 896, wo_voff, dst + j * 1024);
  };
  if (w < 7) { issue_wo(0); issue_wo(1); issue_wo(2); }
#pragma unroll
  for (int n = 0; n < NT; ++n) {
    const float s = rinv[n];
    u32x4 v = {cvtpk(oacc[0][n][0] * s, oacc[0][n][1] * s), cvtpk(oacc[0][n][2] * s, oacc[0][n][3] * s),
               cvtpk(oacc[1][n][0] * s, oacc[1][n][1] * s), cvtpk(oacc[1][n][2] * s, oacc[1][n][3] * s)};
    *(u32x4*)(smem + pw + n * 8192) = v;
  }
  bar();
  if (w < 7) {
    const int swo = (2 * g + (q4 >> 1)) & 3;
    unsigned oa[4];
#pragma unroll
    for (int i = 0; i < 4; ++i) oa[i] = woring + (4 * g + q4) * 128 + ((i ^ swo) << 5) + p4 * 8;
    const unsigned rowb = LDS_A + l15 * 512;
    f32x4 facc[4][NT];
#pragma unroll
    for (int i = 0; i < 4; ++i)
#pragma unroll
      for (int n = 0; n < NT; ++n) facc[i][n] = (f32x4){0.f, 0.f, 0.f, 0.f};
    bf16x8 af[2][4], bo[2][3];
    auto loada = [&](bf16x8 (&d)[4], int ks) {
#pragma unroll
      for (int i = 0; i < 4; ++i) d[i] = tr2(smem + (ks % 3) * 4096, oa[i], oa[i] + 2048);
    };
    auto loadb = [&](bf16x8 (&d)[3], int idx) {
      const int ks = idx / 3, grp = idx % 3;
      const unsigned ca = rowb + (((4 * ks + g) ^ l15) << 4);
#pragma unroll
      for (int j = 0; j < 3; ++j) d[j] = *(const bf16x8*)(smem + ca + (grp * 3 + j) * 8192);
    };
    wait_vm<8>();
    loada(af[0], 0);
    loadb(bo[0], 0);
#pragma unroll
    for (int idx = 0; idx < 24; ++idx) {
      const int ks = idx / 3, grp = idx % 3;
      if (grp == 0 && ks > 0 && ks + 2 < 8) { WAIT_LGKM0(); issue_wo(ks + 2); }
      if (grp == 1 && ks < 7) {
        if (ks < 6) wait_vm<4>(); else wait_vm<0>();
        loada(af[(ks + 1) & 1], ks + 1);
      }
      if (idx + 1 < 24) loadb(bo[(idx + 1) & 1], idx + 1);
      SB();
#pragma unroll
      for (int j = 0; j < 3; ++j)
#pragma unroll
        for (int i = 0; i < 4; ++i) facc[i][grp * 3 + j] = MFMA(af[ks & 1][i], bo[idx & 1][j], facc[i][grp * 3 + j]);
      SB();
    }
    float* pp = partial + (size_t)((p * NB + b) * NH + h) * (252 * 256) + lane * 4;
#pragma unroll
    for (int i = 0; i < 4; ++i)
#pragma unroll
      for (int n = 0; n < NT; ++n) *(f32x4*)(pp + (n * 28 + 4 * w + i) * 256) = facc[i][n];
  }
}

__global__ __launch_bounds__(256) void reduce_heads(const float* __restrict__ partial, __hip_bfloat16* __restrict__ out) {
  const int idx = blockIdx.x * 256 + threadIdx.x;
  const int lane = idx & 63;
  const int T = (idx >> 6) % 252;
  const int pb = (idx >> 6) / 252;
  if (pb >= 16) return;
  const float* src = partial + (size_t)pb * NH * (252 * 256) + T * 256 + lane * 4;
  f32x4 s = {0.f, 0.f, 0.f, 0.f};
#pragma unroll
  for (int hh = 0; hh < NH; ++hh) s += *(const f32x4*)(src + (size_t)hh * (252 * 256));
  const int n = T / 28, mt = T % 28;
  const int p = pb >> 3, b = pb & 7;
  const int row = 16 * n + (lane & 15), col = 16 * mt + 4 * (lane >> 4);
  u32x2 v = {cvtpk(s[0], s[1]), cvtpk(s[2], s[3])};
  *(u32x2*)((char*)out + ((size_t)(b * SQ + row) * (2 * DM) + p * DM + col) * 2) = v;
}

}

extern "C" void kernel_launch(void* const* d_in, const int* in_sizes, int n_in,
                              void* d_out, int out_size, void* d_ws, size_t ws_size,
                              hipStream_t stream) {
  (void)in_sizes; (void)n_in; (void)out_size; (void)ws_size;
  (void)hipFuncSetAttribute((const void*)fused_attn, hipFuncAttributeMaxDynamicSharedMemorySize, LDS_TOTAL);
  float* partial = (float*)d_ws;
  fused_attn<<<256, 512, LDS_TOTAL, stream>>>(
      (const __hip_bfloat16*)d_in[0], (const __hip_bfloat16*)d_in[1], (const __hip_bfloat16*)d_in[2],
      (const __hip_bfloat16*)d_in[3], (const __hip_bfloat16*)d_in[4],
      (const __hip_bfloat16*)d_in[5], (const __hip_bfloat16*)d_in[6], (const __hip_bfloat16*)d_in[7],
      (const __hip_bfloat16*)d_in[8], (const __hip_bfloat16*)d_in[9], partial);
  reduce_heads<<<(16 * 252 * 64 + 255) / 256, 256, 0, stream>>>(partial, (__hip_bfloat16*)d_out);
}
```

```cpp
#include <hip/hip_runtime.h>
#include <hip/hip_bf16.h>
#include <stdint.h>


namespace {

typedef __attribute__((ext_vector_type(8))) short bf16x8;
typedef __attribute__((ext_vector_type(4))) short s16x4;
typedef __attribute__((ext_vector_type(4))) float f32x4;
typedef __attribute__((ext_vector_type(2))) float f32x2;
typedef __attribute__((ext_vector_type(4))) unsigned u32x4;
typedef __attribute__((ext_vector_type(2))) unsigned u32x2;
typedef __attribute__((ext_vector_type(2))) __bf16 bf16x2_t;
typedef __attribute__((address_space(3))) s16x4* lds_tr_ptr;

constexpr int NB = 8, SQ = 144, DM = 448, NH = 16, SK = 512, DH = 256;
constexpr int NT = 9;

constexpr int LDS_A = 0;
constexpr int LDS_B = 73728;
constexpr int LDS_STAT = 155648;
constexpr int LDS_TOTAL = 163840;
constexpr int QSTAGE = 51200;
constexpr int QX = 18432;

__device__ __forceinline__ void dma16(const void* sbase, unsigned voff, unsigned lds_dst) {
  unsigned keep;
  asm volatile("s_mov_b32 %0, m0\n\ts_mov_b32 m0, %3\n\ts_nop 0\n\tglobal_load_lds_dwordx4 %1, %2\n\ts_mov_b32 m0, %0"
               : "=&s"(keep) : "v"(voff), "s"(sbase), "s"(lds_dst) : "memory");
}
__device__ __forceinline__ void dma16nt(const void* sbase, unsigned voff, unsigned lds_dst) {
  unsigned keep;
  asm volatile("s_mov_b32 %0, m0\n\ts_mov_b32 m0, %3\n\ts_nop 0\n\tglobal_load_lds_dwordx4 %1, %2 nt\n\ts_mov_b32 m0, %0"
               : "=&s"(keep) : "v"(voff), "s"(sbase), "s"(lds_dst) : "memory");
}
template <int N> __device__ __forceinline__ void wait_vm() { asm volatile("s_waitcnt vmcnt(%0)" ::"i"(N) : "memory"); }
#define WAIT_LGKM0() asm volatile("s_waitcnt lgkmcnt(0)" ::: "memory")
#define SB() __builtin_amdgcn_sched_barrier(0)
__device__ __forceinline__ void bar() { WAIT_LGKM0(); __builtin_amdgcn_s_barrier(); asm volatile("" ::: "memory"); }

__device__ __forceinline__ unsigned cvtpk(float lo, float hi) {
  f32x2 v = {lo, hi};
  bf16x2_t r = __builtin_convertvector(v, bf16x2_t);
  return __builtin_bit_cast(unsigned, r);
}
__device__ __forceinline__ bf16x8 tr2(const char* base, unsigned a0, unsigned a1) {
  s16x4 l = __builtin_amdgcn_ds_read_tr16_b64_v4i16((lds_tr_ptr)(base + a0));
  s16x4 h = __builtin_amdgcn_ds_read_tr16_b64_v4i16((lds_tr_ptr)(base + a1));
  return (bf16x8){l[0], l[1], l[2], l[3], h[0], h[1], h[2], h[3]};
}
__device__ __forceinline__ float xmax(float v) {
  auto a = __builtin_amdgcn_permlane16_swap(__float_as_uint(v), __float_as_uint(v), false, false);
  v = fmaxf(__uint_as_float(a[0]), __uint_as_float(a[1]));
  auto b = __builtin_amdgcn_permlane32_swap(__float_as_uint(v), __float_as_uint(v), false, false);
  return fmaxf(__uint_as_float(b[0]), __uint_as_float(b[1]));
}
__device__ __forceinline__ float xsum(float v) {
  auto a = __builtin_amdgcn_permlane16_swap(__float_as_uint(v), __float_as_uint(v), false, false);
  v = __uint_as_float(a[0]) + __uint_as_float(a[1]);
  auto b = __builtin_amdgcn_permlane32_swap(__float_as_uint(v), __float_as_uint(v), false, false);
  return __uint_as_float(b[0]) + __uint_as_float(b[1]);
}
template <int NM, int NR> __device__ __forceinline__ void ilv() {
#pragma unroll
  for (int i = 0; i < NM; ++i) {
    __builtin_amdgcn_sched_group_barrier(0x008, 1, 0);
    if (i < NR) __builtin_amdgcn_sched_group_barrier(0x100, 1, 0);
  }
}
#define MFMA(a, b, c) __builtin_amdgcn_mfma_f32_16x16x32_bf16(a, b, c, 0, 0, 0)

template <int NA, int NN> struct Frag { bf16x8 a[NA]; bf16x8 b[NN]; };
template <int NA, int NN>
__device__ __forceinline__ void mma(f32x4 (&acc)[NA][NN], const Frag<NA, NN>& f) {
#pragma unroll
  for (int n = 0; n < NN; ++n)
#pragma unroll
    for (int m = 0; m < NA; ++m) acc[m][n] = MFMA(f.a[m], f.b[n], acc[m][n]);
}

#define RELANE() int lane = threadIdx.x & 63; asm volatile("" : "+v"(lane)); \
  const int l15 = lane & 15, g = lane >> 4, q4 = (lane >> 2) & 3, p4 = lane & 3; (void)l15; (void)g; (void)q4; (void)p4;
struct Ctx {
  char* smem;
  int lane, w, l15, g, q4, p4, hi;
  const char *xb, *kh, *vh, *wqh, *woh;
};

template <int NN, int XCNT>
__device__ __forceinline__ void phase_q(const Ctx& c, int nb) {
  char* smem = c.smem;
  const int w = c.w, lane = c.lane, g = c.g, l15 = c.l15, q4 = c.q4, p4 = c.p4, hi = c.hi, cc = c.w & 3;
  const unsigned x_voff = (unsigned)((8 * w + (lane >> 3)) * 896 + (((lane & 7) ^ ((((w & 1) << 2) + g) & 7)) << 4));
  unsigned wq_voff[4];
#pragma unroll
  for (int jj = 0; jj < 4; ++jj) {
    const int r = 2 * jj + hi;
    const int f = (r & 3) | ((w & 1) << 2);
    wq_voff[jj] = (unsigned)((8 * w + r) * 8192 + (((lane & 31) ^ (f << 1)) << 4));
  }
  auto issue = [&](int s, int buf) {
    const char* xs = c.xb + s * 128;
    const unsigned dst = buf * QSTAGE;
    dma16(xs, x_voff, dst + w * 1024);
    dma16(xs + 64 * 896, x_voff, dst + (w + 8) * 1024);
    if (XCNT == 3) dma16(xs + 128 * 896, x_voff, dst + (w + 16) * 1024);
    const char* ws = c.wqh + (size_t)s * 64 * 8192;
#pragma unroll
    for (int jj = 0; jj < 4; ++jj) dma16(ws, wq_voff[jj], dst + QX + (4 * w + jj) * 1024);
  };
  constexpr int CNT = XCNT + 4;
  const int ftr = q4 | ((g & 1) << 2);
  unsigned qa[4];
#pragma unroll
  for (int m = 0; m < 4; ++m) qa[m] = QX + (8 * g + q4) * 512 + (((4 * cc + m) ^ ftr) << 5) + p4 * 8;
  const unsigned xr0 = (nb * 16 + l15) * 128 + ((g ^ (l15 >> 1)) << 4);
  const unsigned xr1 = xr0 ^ 64;
  auto load = [&](Frag<4, NN>& f, int buf, int ks) {
    const char* sb = smem + buf * QSTAGE;
#pragma unroll
    for (int m = 0; m < 4; ++m) f.a[m] = tr2(sb, qa[m] + ks * 16384, qa[m] + ks * 16384 + 2048);
#pragma unroll
    for (int n = 0; n < NN; ++n) f.b[n] = *(const bf16x8*)(sb + (ks ? xr1 : xr0) + n * 2048);
  };
  f32x4 acc[4][NN];
#pragma unroll
  for (int m = 0; m < 4; ++m)
#pragma unroll
    for (int n = 0; n < NN; ++n) acc[m][n] = (f32x4){0.f, 0.f, 0.f, 0.f};
  Frag<4, NN> f0, f1;
  issue(0, 0); issue(1, 1); issue(2, 2);
  wait_vm<2 * CNT>();
  bar();
  load(f0, 0, 0);
#pragma unroll
  for (int s = 0; s < 7; ++s) {
    const int buf = s % 3, nbuf = (s + 1) % 3;
    load(f1, buf, 1);
    mma<4, NN>(acc, f0);
    ilv<4 * NN, 8 + NN>();
    SB();
    if (s < 6) {
      if (s < 5) wait_vm<CNT>(); else wait_vm<0>();
      bar();
      if (s + 3 < 7) issue(s + 3, buf);
      load(f0, nbuf, 0);
    }
    mma<4, NN>(acc, f1);
    if (s < 6) ilv<4 * NN, 8 + NN>();
    SB();
  }
  bar();
#pragma unroll
  for (int m = 0; m < 4; ++m)
#pragma unroll
    for (int n = 0; n < NN; ++n) {
      u32x2 v = {cvtpk(acc[m][n][0], acc[m][n][1]), cvtpk(acc[m][n][2], acc[m][n][3])};
      const unsigned a = (16 * (nb + n) + l15) * 512 + (((8 * cc + 2 * m + (g >> 1)) ^ l15) << 4) + (g & 1) * 8;
      *(u32x2*)(smem + LDS_A + a) = v;
    }
}

__global__ __launch_bounds__(512, 2) void fused_attn(
    const __hip_bfloat16* __restrict__ xA, const __hip_bfloat16* __restrict__ kA, const __hip_bfloat16* __restrict__ vA,
    const __hip_bfloat16* __restrict__ wqA, const __hip_bfloat16* __restrict__ woA,
    const __hip_bfloat16* __restrict__ xB, const __hip_bfloat16* __restrict__ kB, const __hip_bfloat16* __restrict__ vB,
    const __hip_bfloat16* __restrict__ wqB, const __hip_bfloat16* __restrict__ woB,
    float* __restrict__ partial) {
  extern __shared__ __attribute__((aligned(16))) char smem[];
  const int tid = threadIdx.x;
  Ctx c;
  c.smem = smem;
  c.lane = tid & 63;
  c.w = __builtin_amdgcn_readfirstlane(tid >> 6);
  c.l15 = c.lane & 15; c.g = c.lane >> 4; c.q4 = (c.lane >> 2) & 3; c.p4 = c.lane & 3; c.hi = c.lane >> 5;
  const int w = c.w;

  const int bid = blockIdx.x;
  const int xcd = bid & 7, rr = bid >> 3;
  const int p = xcd >> 2, h = (xcd & 3) * 4 + (rr >> 3), b = rr & 7;

  c.xb = (const char*)((p ? xB : xA) + (size_t)b * SQ * DM);
  c.kh = (const char*)((p ? kB : kA) + (size_t)(b * NH + h) * SK * DH);
  c.vh = (const char*)((p ? vB : vA) + (size_t)(b * NH + h) * SK * DH);
  c.wqh = (const char*)((p ? wqB : wqA) + h * DH);
  c.woh = (const char*)((p ? woB : woA) + (size_t)h * DH * DM);

  if (w < 2) phase_q<5, 3>(c, 0); else if (w < 4) phase_q<5, 2>(c, 0); else phase_q<4, 2>(c, 5);
  unsigned v_voff;
  { RELANE(); v_voff = (unsigned)((lane >> 2) * 512 + 64 * w + (((lane & 3) ^ ((g & 1) << 1)) << 4)); }
  const unsigned vring = LDS_B + w * 10240;
  auto issue_v = [&](int t) {
    const char* src = c.vh + (size_t)(64 * (t & 7) + 32 * (t >> 3)) * 512;
    dma16nt(src, v_voff, vring + (t % 5) * 2048);
    dma16nt(src + 16 * 512, v_voff, vring + (t % 5) * 2048 + 1024);
  };
  issue_v(0); issue_v(1); issue_v(2); issue_v(3); issue_v(4);
  bar();
  f32x4 sacc[4][NT];
#pragma unroll
  for (int m = 0; m < 4; ++m)
#pragma unroll
    for (int n = 0; n < NT; ++n) sacc[m][n] = (f32x4){0.f, 0.f, 0.f, 0.f};
  {
    RELANE();
    const char* kp = c.kh + (size_t)(64 * w + l15) * 512 + g * 16;
    const unsigned rowb = LDS_A + l15 * 512;
    bf16x8 kf[3][4];
#pragma unroll
    for (int ks = 0; ks < 3; ++ks)
#pragma unroll
      for (int m = 0; m < 4; ++m) kf[ks][m] = __builtin_nontemporal_load((const bf16x8*)(kp + m * 16 * 512 + ks * 64));
    bf16x8 bq[2][3];
    auto loadq = [&](bf16x8 (&d)[3], int idx) {
      const int ks = idx / 3, grp = idx % 3;
      const unsigned ca = rowb + (((4 * ks + g) ^ l15) << 4);
#pragma unroll
      for (int j = 0; j < 3; ++j) d[j] = *(const bf16x8*)(smem + ca + (grp * 3 + j) * 8192);
    };
    loadq(bq[0], 0);
#pragma unroll
    for (int idx = 0; idx < 24; ++idx) {
      const int ks = idx / 3, grp = idx % 3;
      if (idx + 1 < 24) loadq(bq[(idx + 1) & 1], idx + 1);
      SB();
#pragma unroll
      for (int j = 0; j < 3; ++j)
#pragma unroll
        for (int m = 0; m < 4; ++m) sacc[m][grp * 3 + j] = MFMA(kf[ks % 3][m], bq[idx & 1][j], sacc[m][grp * 3 + j]);
      if (grp == 2 && ks + 3 < 8) {
#pragma unroll
        for (int m = 0; m < 4; ++m) kf[ks % 3][m] = __builtin_nontemporal_load((const bf16x8*)(kp + m * 16 * 512 + (ks + 3) * 64));
      }
      SB();
    }
  }
  float* smax = (float*)(smem + LDS_STAT);
  float* ssum = (float*)(smem + LDS_STAT);
  u32x4 pk1[NT];
  {
  RELANE();
#pragma unroll
  for (int n = 0; n < NT; ++n) {
    float m0 = fmaxf(fmaxf(sacc[0][n][0], sacc[0][n][1]), fmaxf(sacc[0][n][2], sacc[0][n][3]));
    float m1 = fmaxf(fmaxf(sacc[1][n][0], sacc[1][n][1]), fmaxf(sacc[1][n][2], sacc[1][n][3]));
    float m2 = fmaxf(fmaxf(sacc[2][n][0], sacc[2][n][1]), fmaxf(sacc[2][n][2], sacc[2][n][3]));
    float m3 = fmaxf(fmaxf(sacc[3][n][0], sacc[3][n][1]), fmaxf(sacc[3][n][2], sacc[3][n][3]));
    const float m = xmax(fmaxf(fmaxf(m0, m1), fmaxf(m2, m3)));
    smax[(16 * n + l15) * 8 + w] = m;
  }
  bar();
  constexpr float CEXP = 0.0625f * 1.4426950408889634f;
  float mc[NT];
#pragma unroll
  for (int n = 0; n < NT; ++n) {
    const f32x4 m0 = *(const f32x4*)(smax + (16 * n + l15) * 8);
    const f32x4 m1 = *(const f32x4*)(smax + (16 * n + l15) * 8 + 4);
    mc[n] = -CEXP * fmaxf(fmaxf(fmaxf(m0[0], m0[1]), fmaxf(m0[2], m0[3])), fmaxf(fmaxf(m1[0], m1[1]), fmaxf(m1[2], m1[3])));
  }
  float rsum[NT];
  const unsigned pw = LDS_A + l15 * 512 + (((4 * w + g) ^ l15) << 4);
#pragma unroll
  for (int n = 0; n < NT; ++n) {
    float e[4][4];
#pragma unroll
    for (int mm = 0; mm < 4; ++mm)
#pragma unroll
      for (int r = 0; r < 4; ++r) e[mm][r] = __builtin_amdgcn_exp2f(fmaf(sacc[mm][n][r], CEXP, mc[n]));
    const float s0 = (e[0][0] + e[0][1]) + (e[0][2] + e[0][3]), s1 = (e[1][0] + e[1][1]) + (e[1][2] + e[1][3]);
    const float s2 = (e[2][0] + e[2][1]) + (e[2][2] + e[2][3]), s3 = (e[3][0] + e[3][1]) + (e[3][2] + e[3][3]);
    rsum[n] = xsum((s0 + s1) + (s2 + s3));
    u32x4 p0 = {cvtpk(e[0][0], e[0][1]), cvtpk(e[0][2], e[0][3]), cvtpk(e[1][0], e[1][1]), cvtpk(e[1][2], e[1][3])};
    *(u32x4*)(smem + pw + n * 8192) = p0;
    pk1[n] = (u32x4){cvtpk(e[2][0], e[2][1]), cvtpk(e[2][2], e[2][3]), cvtpk(e[3][0], e[3][1]), cvtpk(e[3][2], e[3][3])};
  }
  bar();
#pragma unroll
  for (int n = 0; n < NT; ++n) ssum[(16 * n + l15) * 8 + w] = rsum[n];
  }
  f32x4 oacc[2][NT];
#pragma unroll
  for (int m = 0; m < 2; ++m)
#pragma unroll
    for (int n = 0; n < NT; ++n) oacc[m][n] = (f32x4){0.f, 0.f, 0.f, 0.f};
  {
    RELANE();
    const unsigned pw = LDS_A + l15 * 512 + (((4 * w + g) ^ l15) << 4);
    const unsigned va0 = vring + (4 * g + q4) * 64 + ((0 ^ (g & 1)) << 5) + p4 * 8;
    const unsigned va1 = vring + (4 * g + q4) * 64 + ((1 ^ (g & 1)) << 5) + p4 * 8;
    const unsigned rowb = LDS_A + l15 * 512;
    auto load = [&](Frag<2, NT>& f, int t) {
      const char* vs = smem + (t % 5) * 2048;
      f.a[0] = tr2(vs, va0, va0 + 1024);
      f.a[1] = tr2(vs, va1, va1 + 1024);
      const unsigned ca = rowb + (((4 * (t & 7) + g) ^ l15) << 4);
#pragma unroll
      for (int n = 0; n < NT; ++n) f.b[n] = *(const bf16x8*)(smem + ca + n * 8192);
    };
    Frag<2, NT> f0, f1;
    wait_vm<8>();
    load(f0, 0);
    auto step = [&](Frag<2, NT>& cur, Frag<2, NT>& nxt, int t) {
      if (t < 15) {
        WAIT_LGKM0();
        if (t + 5 < 16) issue_v(t + 5);
        if (t + 5 < 16) wait_vm<8>(); else if (t == 11) wait_vm<6>(); else if (t == 12) wait_vm<4>(); else if (t == 13) wait_vm<2>(); else wait_vm<0>();
        if (t == 7) {
          bar();
#pragma unroll
          for (int n = 0; n < NT; ++n) *(u32x4*)(smem + pw + n * 8192) = pk1[n];
          bar();
        }
        load(nxt, t + 1);
      }
      mma<2, NT>(oacc, cur);
      if (t < 15) ilv<18, 13>();
      SB();
    };
#pragma unroll
    for (int t = 0; t < 16; t += 2) { step(f0, f1, t); step(f1, f0, t + 1); }
  }
  RELANE();
  const unsigned pw = LDS_A + l15 * 512 + (((4 * w + g) ^ l15) << 4);
  float rinv[NT];
#pragma unroll
  for (int n = 0; n < NT; ++n) {
    const f32x4 s0 = *(const f32x4*)(ssum + (16 * n + l15) * 8);
    const f32x4 s1 = *(const f32x4*)(ssum + (16 * n + l15) * 8 + 4);
    rinv[n] = 1.0f / (((s0[0] + s0[1]) + (s0[2] + s0[3])) + ((s1[0] + s1[1]) + (s1[2] + s1[3])));
  }
  bar();

  const unsigned wo_voff = (unsigned)((lane >> 3) * 896 + 128 * w + (((lane & 7) ^ (g << 1)) << 4));
  const unsigned woring = LDS_B + w * 12288;
  auto issue_wo = [&](int ks) {
    const char* src = c.woh + (size_t)ks * 32 * 896;
    const unsigned dst = woring + (ks % 3) * 4096;
#pragma unroll
    for (int j = 0; j < 4; ++j) dma16(src + j * 8 * 896, wo_voff, dst + j * 1024);
  };
  if (w < 7) { issue_wo(0); issue_wo(1); issue_wo(2); }
#pragma unroll
  for (int n = 0; n < NT; ++n) {
    const float s = rinv[n];
    u32x4 v = {cvtpk(oacc[0][n][0] * s, oacc[0][n][1] * s), cvtpk(oacc[0][n][2] * s, oacc[0][n][3] * s),
               cvtpk(oacc[1][n][0] * s, oacc[1][n][1] * s), cvtpk(oacc[1][n][2] * s, oacc[1][n][3] * s)};
    *(u32x4*)(smem + pw + n * 8192) = v;
  }
  bar();
  if (w < 7) {
    const int swo = (2 * g + (q4 >> 1)) & 3;
    unsigned oa[4];
#pragma unroll
    for (int i = 0; i < 4; ++i) oa[i] = woring + (4 * g + q4) * 128 + ((i ^ swo) << 5) + p4 * 8;
    const unsigned rowb = LDS_A + l15 * 512;
    f32x4 facc[4][NT];
#pragma unroll
    for (int i = 0; i < 4; ++i)
#pragma unroll
      for (int n = 0; n < NT; ++n) facc[i][n] = (f32x4){0.f, 0.f, 0.f, 0.f};
    bf16x8 af[2][4], bo[2][3];
    auto loada = [&](bf16x8 (&d)[4], int ks) {
#pragma unroll
      for (int i = 0; i < 4; ++i) d[i] = tr2(smem + (ks % 3) * 4096, oa[i], oa[i] + 2048);
    };
    auto loadb = [&](bf16x8 (&d)[3], int idx) {
      const int ks = idx / 3, grp = idx % 3;
      const unsigned ca = rowb + (((4 * ks + g) ^ l15) << 4);
#pragma unroll
      for (int j = 0; j < 3; ++j) d[j] = *(const bf16x8*)(smem + ca + (grp * 3 + j) * 8192);
    };
    wait_vm<8>();
    loada(af[0], 0);
    loadb(bo[0], 0);
#pragma unroll
    for (int idx = 0; idx < 24; ++idx) {
      const int ks = idx / 3, grp = idx % 3;
      if (grp == 0 && ks > 0 && ks + 2 < 8) { WAIT_LGKM0(); issue_wo(ks + 2); }
      if (grp == 1 && ks < 7) {
        if (ks < 6) wait_vm<4>(); else wait_vm<0>();
        loada(af[(ks + 1) & 1], ks + 1);
      }
      if (idx + 1 < 24) loadb(bo[(idx + 1) & 1], idx + 1);
      SB();
#pragma unroll
      for (int j = 0; j < 3; ++j)
#pragma unroll
        for (int i = 0; i < 4; ++i) facc[i][grp * 3 + j] = MFMA(af[ks & 1][i], bo[idx & 1][j], facc[i][grp * 3 + j]);
      SB();
    }
    float* pp = partial + (size_t)((p * NB + b) * NH + h) * (252 * 256) + lane * 4;
#pragma unroll
    for (int i = 0; i < 4; ++i)
#pragma unroll
      for (int n = 0; n < NT; ++n) *(f32x4*)(pp + (n * 28 + 4 * w + i) * 256) = facc[i][n];
  }
}

__global__ __launch_bounds__(256) void reduce_heads(const float* __restrict__ partial, __hip_bfloat16* __restrict__ out) {
  const int idx = blockIdx.x * 256 + threadIdx.x;
  const int lane = idx & 63;
  const int T = (idx >> 6) % 252;
  const int pb = (idx >> 6) / 252;
  if (pb >= 16) return;
  const float* src = partial + (size_t)pb * NH * (252 * 256) + T * 256 + lane * 4;
  f32x4 s = {0.f, 0.f, 0.f, 0.f};
#pragma unroll
  for (int hh = 0; hh < NH; ++hh) s += *(const f32x4*)(src + (size_t)hh * (252 * 256));
  const int n = T / 28, mt = T % 28;
  const int p = pb >> 3, b = pb & 7;
  const int row = 16 * n + (lane & 15), col = 16 * mt + 4 * (lane >> 4);
  u32x2 v = {cvtpk(s[0], s[1]), cvtpk(s[2], s[3])};
  *(u32x2*)((char*)out + ((size_t)(b * SQ + row) * (2 * DM) + p * DM + col) * 2) = v;
}

}

extern "C" void kernel_launch(void* const* d_in, const int* in_sizes, int n_in,
                              void* d_out, int out_size, void* d_ws, size_t ws_size,
                              hipStream_t stream) {
  (void)in_sizes; (void)n_in; (void)out_size; (void)ws_size;
  (void)hipFuncSetAttribute((const void*)fused_attn, hipFuncAttributeMaxDynamicSharedMemorySize, LDS_TOTAL);
  float* partial = (float*)d_ws;
  fused_attn<<<256, 512, LDS_TOTAL, stream>>>(
      (const __hip_bfloat16*)d_in[0], (const __hip_bfloat16*)d_in[1], (const __hip_bfloat16*)d_in[2],
      (const __hip_bfloat16*)d_in[3], (const __hip_bfloat16*)d_in[4],
      (const __hip_bfloat16*)d_in[5], (const __hip_bfloat16*)d_in[6], (const __hip_bfloat16*)d_in[7],
      (const __hip_bfloat16*)d_in[8], (const __hip_bfloat16*)d_in[9], partial);
  reduce_heads<<<(16 * 252 * 64 + 255) / 256, 256, 0, stream>>>(partial, (__hip_bfloat16*)d_out);
}
```

```cpp
#include <hip/hip_runtime.h>
#include <hip/hip_bf16.h>
#include <stdint.h>


namespace {

typedef __attribute__((ext_vector_type(8))) short bf16x8;
typedef __attribute__((ext_vector_type(4))) short s16x4;
typedef __attribute__((ext_vector_type(4))) float f32x4;
typedef __attribute__((ext_vector_type(2))) float f32x2;
typedef __attribute__((ext_vector_type(4))) unsigned u32x4;
typedef __attribute__((ext_vector_type(2))) unsigned u32x2;
typedef __attribute__((ext_vector_type(2))) __bf16 bf16x2_t;
typedef __attribute__((address_space(3))) s16x4* lds_tr_ptr;

constexpr int NB = 8, SQ = 144, DM = 448, NH = 16, SK = 512, DH = 256;
constexpr int NT = 9;

constexpr int LDS_A = 0;
constexpr int LDS_B = 73728;
constexpr int LDS_STAT = 155648;
constexpr int LDS_TOTAL = 163840;
constexpr int QSTAGE = 51200;
constexpr int QX = 18432;

__device__ __forceinline__ void dma16(const void* sbase, unsigned voff, unsigned lds_dst) {
  unsigned keep;
  asm volatile("s_mov_b32 %0, m0\n\ts_mov_b32 m0, %3\n\ts_nop 0\n\tglobal_load_lds_dwordx4 %1, %2\n\ts_mov_b32 m0, %0"
               : "=&s"(keep) : "v"(voff), "s"(sbase), "s"(lds_dst) : "memory");
}
__device__ __forceinline__ bf16x8 gload16(const void* sbase, unsigned voff) {
  bf16x8 r;
  asm volatile("global_load_dwordx4 %0, %1, %2" : "=v"(r) : "v"(voff), "s"(sbase) : "memory");
  return r;
}
#define PIN4(a) asm volatile("" : "+v"(a[0]), "+v"(a[1]), "+v"(a[2]), "+v"(a[3]))
template <int N> __device__ __forceinline__ void wait_vm() { asm volatile("s_waitcnt vmcnt(%0)" ::"i"(N) : "memory"); }
#define WAIT_LGKM0() asm volatile("s_waitcnt lgkmcnt(0)" ::: "memory")
#define SB() __builtin_amdgcn_sched_barrier(0)
__device__ __forceinline__ void bar() { WAIT_LGKM0(); __builtin_amdgcn_s_barrier(); asm volatile("" ::: "memory"); }

__device__ __forceinline__ unsigned cvtpk(float lo, float hi) {
  f32x2 v = {lo, hi};
  bf16x2_t r = __builtin_convertvector(v, bf16x2_t);
  return __builtin_bit_cast(unsigned, r);
}
__device__ __forceinline__ bf16x8 tr2(const char* base, unsigned a0, unsigned a1) {
  s16x4 l = __builtin_amdgcn_ds_read_tr16_b64_v4i16((lds_tr_ptr)(base + a0));
  s16x4 h = __builtin_amdgcn_ds_read_tr16_b64_v4i16((lds_tr_ptr)(base + a1));
  return (bf16x8){l[0], l[1], l[2], l[3], h[0], h[1], h[2], h[3]};
}
__device__ __forceinline__ float xmax(float v) {
  auto a = __builtin_amdgcn_permlane16_swap(__float_as_uint(v), __float_as_uint(v), false, false);
  v = fmaxf(__uint_as_float(a[0]), __uint_as_float(a[1]));
  auto b = __builtin_amdgcn_permlane32_swap(__float_as_uint(v), __float_as_uint(v), false, false);
  return fmaxf(__uint_as_float(b[0]), __uint_as_float(b[1]));
}
__device__ __forceinline__ float xsum(float v) {
  auto a = __builtin_amdgcn_permlane16_swap(__float_as_uint(v), __float_as_uint(v), false, false);
  v = __uint_as_float(a[0]) + __uint_as_float(a[1]);
  auto b = __builtin_amdgcn_permlane32_swap(__float_as_uint(v), __float_as_uint(v), false, false);
  return __uint_as_float(b[0]) + __uint_as_float(b[1]);
}
template <int NM, int NR> __device__ __forceinline__ void ilv() {
#pragma unroll
  for (int i = 0; i < NM; ++i) {
    __builtin_amdgcn_sched_group_barrier(0x008, 1, 0);
    if (i < NR) __builtin_amdgcn_sched_group_barrier(0x100, 1, 0);
  }
}
#define MFMA(a, b, c) __builtin_amdgcn_mfma_f32_16x16x32_bf16(a, b, c, 0, 0, 0)

template <int NA, int NN> struct Frag { bf16x8 a[NA]; bf16x8 b[NN]; };
template <int NA, int NN>
__device__ __forceinline__ void mma(f32x4 (&acc)[NA][NN], const Frag<NA, NN>& f) {
#pragma unroll
  for (int n = 0; n < NN; ++n)
#pragma unroll
    for (int m = 0; m < NA; ++m) acc[m][n] = MFMA(f.a[m], f.b[n], acc[m][n]);
}

#define RELANE() int lane = threadIdx.x & 63; asm volatile("" : "+v"(lane)); \
  const int l15 = lane & 15, g = lane >> 4, q4 = (lane >> 2) & 3, p4 = lane & 3; (void)l15; (void)g; (void)q4; (void)p4;
struct Ctx {
  char* smem;
  int lane, w, l15, g, q4, p4, hi;
  const char *xb, *kh, *vh, *wqh, *woh;
};

constexpr int PSZ(int i) { return i < 3 ? 4 : 2; }
template <int NN, int XCNT, class PRE>
__device__ __forceinline__ void phase_q(const Ctx& c, int nb, PRE pre) {
  char* smem = c.smem;
  const int w = c.w, lane = c.lane, g = c.g, l15 = c.l15, q4 = c.q4, p4 = c.p4, hi = c.hi, cc = c.w & 3;
  const unsigned x_voff = (unsigned)((8 * w + (lane >> 3)) * 896 + (((lane & 7) ^ ((((w & 1) << 2) + g) & 7)) << 4));
  unsigned wq_voff[4];
#pragma unroll
  for (int jj = 0; jj < 4; ++jj) {
    const int r = 2 * jj + hi;
    const int f = (r & 3) | ((w & 1) << 2);
    wq_voff[jj] = (unsigned)((8 * w + r) * 8192 + (((lane & 31) ^ (f << 1)) << 4));
  }
  auto issue = [&](int s) {
    const char* xs = c.xb + s * 128;
    const unsigned dst = (s & 1) * QSTAGE;
    dma16(xs, x_voff, dst + w * 1024);
    dma16(xs + 64 * 896, x_voff, dst + (w + 8) * 1024);
    if (XCNT == 3) dma16(xs + 128 * 896, x_voff, dst + (w + 16) * 1024);
    const char* ws = c.wqh + (size_t)s * 64 * 8192;
#pragma unroll
    for (int jj = 0; jj < 4; ++jj) dma16(ws, wq_voff[jj], dst + QX + (4 * w + jj) * 1024);
  };
  constexpr int CNT = XCNT + 4;
  const int ftr = q4 | ((g & 1) << 2);
  unsigned qa[4];
#pragma unroll
  for (int m = 0; m < 4; ++m) qa[m] = QX + (8 * g + q4) * 512 + (((4 * cc + m) ^ ftr) << 5) + p4 * 8;
  const unsigned xr0 = (nb * 16 + l15) * 128 + ((g ^ (l15 >> 1)) << 4);
  const unsigned xr1 = xr0 ^ 64;
  auto load = [&](Frag<4, NN>& f, int s, int ks) {
    const char* sb = smem + (s & 1) * QSTAGE;
#pragma unroll
    for (int m = 0; m < 4; ++m) f.a[m] = tr2(sb, qa[m] + ks * 16384, qa[m] + ks * 16384 + 2048);
#pragma unroll
    for (int n = 0; n < NN; ++n) f.b[n] = *(const bf16x8*)(sb + (ks ? xr1 : xr0) + n * 2048);
  };
  f32x4 acc[4][NN];
#pragma unroll
  for (int m = 0; m < 4; ++m)
#pragma unroll
    for (int n = 0; n < NN; ++n) acc[m][n] = (f32x4){0.f, 0.f, 0.f, 0.f};
  Frag<4, NN> f0, f1;
  issue(0); issue(1); pre(0);
  wait_vm<CNT + PSZ(0)>();
  bar();
  load(f0, 0, 0);
#pragma unroll
  for (int s = 0; s < 7; ++s) {
    load(f1, s, 1);
    mma<4, NN>(acc, f0);
    ilv<4 * NN, 8 + NN>();
    SB();
    if (s < 6) {
      if (s == 0) wait_vm<PSZ(0)>(); else if (s == 1) wait_vm<PSZ(1)>(); else if (s == 2) wait_vm<PSZ(2)>();
      else if (s == 3) wait_vm<PSZ(3)>(); else if (s == 4) wait_vm<PSZ(4)>(); else wait_vm<PSZ(5)>();
      bar();
      if (s + 2 < 7) { issue(s + 2); pre(s + 1); }
      load(f0, s + 1, 0);
    }
    mma<4, NN>(acc, f1);
    if (s < 6) ilv<4 * NN, 8 + NN>();
    SB();
  }
  bar();
#pragma unroll
  for (int m = 0; m < 4; ++m)
#pragma unroll
    for (int n = 0; n < NN; ++n) {
      u32x2 v = {cvtpk(acc[m][n][0], acc[m][n][1]), cvtpk(acc[m][n][2], acc[m][n][3])};
      const unsigned a = (16 * (nb + n) + l15) * 512 + (((8 * cc + 2 * m + (g >> 1)) ^ l15) << 4) + (g & 1) * 8;
      *(u32x2*)(smem + LDS_A + a) = v;
    }
}

__global__ __launch_bounds__(512, 2) void fused_attn(
    const __hip_bfloat16* __restrict__ xA, const __hip_bfloat16* __restrict__ kA, const __hip_bfloat16* __restrict__ vA,
    const __hip_bfloat16* __restrict__ wqA, const __hip_bfloat16* __restrict__ woA,
    const __hip_bfloat16* __restrict__ xB, const __hip_bfloat16* __restrict__ kB, const __hip_bfloat16* __restrict__ vB,
    const __hip_bfloat16* __restrict__ wqB, const __hip_bfloat16* __restrict__ woB,
    float* __restrict__ partial) {
  extern __shared__ __attribute__((aligned(16))) char smem[];
  const int tid = threadIdx.x;
  Ctx c;
  c.smem = smem;
  c.lane = tid & 63;
  c.w = __builtin_amdgcn_readfirstlane(tid >> 6);
  c.l15 = c.lane & 15; c.g = c.lane >> 4; c.q4 = (c.lane >> 2) & 3; c.p4 = c.lane & 3; c.hi = c.lane >> 5;
  const int w = c.w;

  const int bid = blockIdx.x;
  const int xcd = bid & 7, rr = bid >> 3;
  const int p = xcd >> 2, h = (xcd & 3) * 4 + (rr >> 3), b = rr & 7;

  c.xb = (const char*)((p ? xB : xA) + (size_t)b * SQ * DM);
  c.kh = (const char*)((p ? kB : kA) + (size_t)(b * NH + h) * SK * DH);
  c.vh = (const char*)((p ? vB : vA) + (size_t)(b * NH + h) * SK * DH);
  c.wqh = (const char*)((p ? wqB : wqA) + h * DH);
  c.woh = (const char*)((p ? woB : woA) + (size_t)h * DH * DM);

  unsigned v_voff, k_voff;
  { RELANE(); v_voff = (unsigned)((lane >> 2) * 512 + 64 * w + (((lane & 3) ^ ((g & 1) << 1)) << 4));
    k_voff = (unsigned)((64 * w + l15) * 512 + g * 16); }
  const unsigned vring = LDS_B + w * 2048;
  auto issue_v = [&](int t) {
    const char* src = c.vh + (size_t)(64 * (t & 7) + 32 * (t >> 3)) * 512;
    dma16(src, v_voff, vring + ((t + 2) % 5) * 16384);
    dma16(src + 16 * 512, v_voff, vring + ((t + 2) % 5) * 16384 + 1024);
  };
  bf16x8 kf[3][4];
  auto load_k = [&](int ks) {
#pragma unroll
    for (int m = 0; m < 4; ++m) kf[ks % 3][m] = gload16(c.kh + m * 16 * 512 + ks * 64, k_voff);
  };
  auto pre = [&](int i) { if (i < 3) load_k(i); else issue_v(i - 3); };
  if (w < 2) phase_q<5, 3>(c, 0, pre); else if (w < 4) phase_q<5, 2>(c, 0, pre); else phase_q<4, 2>(c, 5, pre);
  issue_v(3); issue_v(4);
  bar();
  f32x4 sacc[4][NT];
#pragma unroll
  for (int m = 0; m < 4; ++m)
#pragma unroll
    for (int n = 0; n < NT; ++n) sacc[m][n] = (f32x4){0.f, 0.f, 0.f, 0.f};
  {
    RELANE();
    const unsigned rowb = LDS_A + l15 * 512;
    bf16x8 bq[2][3];
    auto loadq = [&](bf16x8 (&d)[3], int idx) {
      const int ks = idx / 3, grp = idx % 3;
      const unsigned ca = rowb + (((4 * ks + g) ^ l15) << 4);
#pragma unroll
      for (int j = 0; j < 3; ++j) d[j] = *(const bf16x8*)(smem + ca + (grp * 3 + j) * 8192);
    };
    loadq(bq[0], 0);
#pragma unroll
    for (int idx = 0; idx < 24; ++idx) {
      const int ks = idx / 3, grp = idx % 3;
      if (idx + 1 < 24) loadq(bq[(idx + 1) & 1], idx + 1);
      if (grp == 0) {
        if (ks == 0) wait_vm<4>();
        else if (ks >= 3 && ks < 6) wait_vm<8>(); else if (ks == 6) wait_vm<4>(); else if (ks == 7) wait_vm<0>();
        PIN4(kf[ks % 3]);
      }
      SB();
#pragma unroll
      for (int j = 0; j < 3; ++j)
#pragma unroll
        for (int m = 0; m < 4; ++m) sacc[m][grp * 3 + j] = MFMA(kf[ks % 3][m], bq[idx & 1][j], sacc[m][grp * 3 + j]);
      SB();
      if (grp == 2 && ks + 3 < 8) load_k(ks + 3);
    }
  }
  float* smax = (float*)(smem + LDS_STAT);
  float* ssum = (float*)(smem + LDS_STAT);
  u32x4 pk1[NT];
  {
  RELANE();
#pragma unroll
  for (int n = 0; n < NT; ++n) {
    float m0 = fmaxf(fmaxf(sacc[0][n][0], sacc[0][n][1]), fmaxf(sacc[0][n][2], sacc[0][n][3]));
    float m1 = fmaxf(fmaxf(sacc[1][n][0], sacc[1][n][1]), fmaxf(sacc[1][n][2], sacc[1][n][3]));
    float m2 = fmaxf(fmaxf(sacc[2][n][0], sacc[2][n][1]), fmaxf(sacc[2][n][2], sacc[2][n][3]));
    float m3 = fmaxf(fmaxf(sacc[3][n][0], sacc[3][n][1]), fmaxf(sacc[3][n][2], sacc[3][n][3]));
    const float m = xmax(fmaxf(fmaxf(m0, m1), fmaxf(m2, m3)));
    smax[(16 * n + l15) * 8 + w] = m;
  }
  bar();
  constexpr float CEXP = 0.0625f * 1.4426950408889634f;
  float mc[NT];
#pragma unroll
  for (int n = 0; n < NT; ++n) {
    const f32x4 m0 = *(const f32x4*)(smax + (16 * n + l15) * 8);
    const f32x4 m1 = *(const f32x4*)(smax + (16 * n + l15) * 8 + 4);
    mc[n] = -CEXP * fmaxf(fmaxf(fmaxf(m0[0], m0[1]), fmaxf(m0[2], m0[3])), fmaxf(fmaxf(m1[0], m1[1]), fmaxf(m1[2], m1[3])));
  }
  float rsum[NT];
  const unsigned pw = LDS_A + l15 * 512 + (((4 * w + g) ^ l15) << 4);
#pragma unroll
  for (int n = 0; n < NT; ++n) {
    float e[4][4];
#pragma unroll
    for (int mm = 0; mm < 4; ++mm)
#pragma unroll
      for (int r = 0; r < 4; ++r) e[mm][r] = __builtin_amdgcn_exp2f(fmaf(sacc[mm][n][r], CEXP, mc[n]));
    const float s0 = (e[0][0] + e[0][1]) + (e[0][2] + e[0][3]), s1 = (e[1][0] + e[1][1]) + (e[1][2] + e[1][3]);
    const float s2 = (e[2][0] + e[2][1]) + (e[2][2] + e[2][3]), s3 = (e[3][0] + e[3][1]) + (e[3][2] + e[3][3]);
    rsum[n] = xsum((s0 + s1) + (s2 + s3));
    u32x4 p0 = {cvtpk(e[0][0], e[0][1]), cvtpk(e[0][2], e[0][3]), cvtpk(e[1][0], e[1][1]), cvtpk(e[1][2], e[1][3])};
    *(u32x4*)(smem + pw + n * 8192) = p0;
    pk1[n] = (u32x4){cvtpk(e[2][0], e[2][1]), cvtpk(e[2][2], e[2][3]), cvtpk(e[3][0], e[3][1]), cvtpk(e[3][2], e[3][3])};
  }
  bar();
#pragma unroll
  for (int n = 0; n < NT; ++n) ssum[(16 * n + l15) * 8 + w] = rsum[n];
  }
  f32x4 oacc[2][NT];
#pragma unroll
  for (int m = 0; m < 2; ++m)
#pragma unroll
    for (int n = 0; n < NT; ++n) oacc[m][n] = (f32x4){0.f, 0.f, 0.f, 0.f};
  {
    RELANE();
    const unsigned pw = LDS_A + l15 * 512 + (((4 * w + g) ^ l15) << 4);
    const unsigned va0 = vring + (4 * g + q4) * 64 + ((0 ^ (g & 1)) << 5) + p4 * 8;
    const unsigned va1 = vring + (4 * g + q4) * 64 + ((1 ^ (g & 1)) << 5) + p4 * 8;
    const unsigned rowb = LDS_A + l15 * 512;
    auto load = [&](Frag<2, NT>& f, int t) {
      const char* vs = smem + ((t + 2) % 5) * 16384;
      f.a[0] = tr2(vs, va0, va0 + 1024);
      f.a[1] = tr2(vs, va1, va1 + 1024);
      const unsigned ca = rowb + (((4 * (t & 7) + g) ^ l15) << 4);
#pragma unroll
      for (int n = 0; n < NT; ++n) f.b[n] = *(const bf16x8*)(smem + ca + n * 8192);
    };
    Frag<2, NT> f0, f1;
    wait_vm<8>();
    load(f0, 0);
    auto step = [&](Frag<2, NT>& cur, Frag<2, NT>& nxt, int t) {
      if (t < 15) {
        WAIT_LGKM0();
        if (t + 5 < 16) issue_v(t + 5);
        if (t + 5 < 16) wait_vm<8>(); else if (t == 11) wait_vm<6>(); else if (t == 12) wait_vm<4>(); else if (t == 13) wait_vm<2>(); else wait_vm<0>();
        if (t == 7) {
          bar();
#pragma unroll
          for (int n = 0; n < NT; ++n) *(u32x4*)(smem + pw + n * 8192) = pk1[n];
          bar();
        }
        load(nxt, t + 1);
      }
      mma<2, NT>(oacc, cur);
      if (t < 15) ilv<18, 13>();
      SB();
    };
#pragma unroll
    for (int t = 0; t < 16; t += 2) { step(f0, f1, t); step(f1, f0, t + 1); }
  }
  RELANE();
  const unsigned pw = LDS_A + l15 * 512 + (((4 * w + g) ^ l15) << 4);
  float rinv[NT];
#pragma unroll
  for (int n = 0; n < NT; ++n) {
    const f32x4 s0 = *(const f32x4*)(ssum + (16 * n + l15) * 8);
    const f32x4 s1 = *(const f32x4*)(ssum + (16 * n + l15) * 8 + 4);
    rinv[n] = 1.0f / (((s0[0] + s0[1]) + (s0[2] + s0[3])) + ((s1[0] + s1[1]) + (s1[2] + s1[3])));
  }
  bar();

  const unsigned wo_voff = (unsigned)((lane >> 3) * 896 + 128 * w + (((lane & 7) ^ (g << 1)) << 4));
  const unsigned woring = LDS_B + w * 12288;
  auto issue_wo = [&](int ks) {
    const char* src = c.woh + (size_t)ks * 32 * 896;
    const unsigned dst = woring + (ks % 3) * 4096;
#pragma unroll
    for (int j = 0; j < 4; ++j) dma16(src + j * 8 * 896, wo_voff, dst + j * 1024);
  };
  if (w < 7) { issue_wo(0); issue_wo(1); issue_wo(2); }
#pragma unroll
  for (int n = 0; n < NT; ++n) {
    const float s = rinv[n];
    u32x4 v = {cvtpk(oacc[0][n][0] * s, oacc[0][n][1] * s), cvtpk(oacc[0][n][2] * s, oacc[0][n][3] * s),
               cvtpk(oacc[1][n][0] * s, oacc[1][n][1] * s), cvtpk(oacc[1][n][2] * s, oacc[1][n][3] * s)};
    *(u32x4*)(smem + pw + n * 8192) = v;
  }
  bar();
  if (w < 7) {
    const int swo = (2 * g + (q4 >> 1)) & 3;
    unsigned oa[4];
#pragma unroll
    for (int i = 0; i < 4; ++i) oa[i] = woring + (4 * g + q4) * 128 + ((i ^ swo) << 5) + p4 * 8;
    const unsigned rowb = LDS_A + l15 * 512;
    f32x4 facc[4][NT];
#pragma unroll
    for (int i = 0; i < 4; ++i)
#pragma unroll
      for (int n = 0; n < NT; ++n) facc[i][n] = (f32x4){0.f, 0.f, 0.f, 0.f};
    bf16x8 af[2][4], bo[2][3];
    auto loada = [&](bf16x8 (&d)[4], int ks) {
#pragma unroll
      for (int i = 0; i < 4; ++i) d[i] = tr2(smem + (ks % 3) * 4096, oa[i], oa[i] + 2048);
    };
    auto loadb = [&](bf16x8 (&d)[3], int idx) {
      const int ks = idx / 3, grp = idx % 3;
      const unsigned ca = rowb + (((4 * ks + g) ^ l15) << 4);
#pragma unroll
      for (int j = 0; j < 3; ++j) d[j] = *(const bf16x8*)(smem + ca + (grp * 3 + j) * 8192);
    };
    wait_vm<8>();
    loada(af[0], 0);
    loadb(bo[0], 0);
#pragma unroll
    for (int idx = 0; idx < 24; ++idx) {
      const int ks = idx / 3, grp = idx % 3;
      if (grp == 0 && ks > 0 && ks + 2 < 8) { WAIT_LGKM0(); issue_wo(ks + 2); }
      if (grp == 1 && ks < 7) {
        if (ks < 6) wait_vm<4>(); else wait_vm<0>();
        loada(af[(ks + 1) & 1], ks + 1);
      }
      if (idx + 1 < 24) loadb(bo[(idx + 1) & 1], idx + 1);
      SB();
#pragma unroll
      for (int j = 0; j < 3; ++j)
#pragma unroll
        for (int i = 0; i < 4; ++i) facc[i][grp * 3 + j] = MFMA(af[ks & 1][i], bo[idx & 1][j], facc[i][grp * 3 + j]);
      SB();
    }
    char* pp = (char*)partial + (size_t)((p * NB + b) * NH + h) * (126 * 1024) + lane * 16;
#pragma unroll
    for (int i = 0; i < 2; ++i)
#pragma unroll
      for (int n = 0; n < NT; ++n) {
        u32x4 v = {cvtpk(facc[2 * i][n][0], facc[2 * i][n][1]), cvtpk(facc[2 * i][n][2], facc[2 * i][n][3]),
                   cvtpk(facc[2 * i + 1][n][0], facc[2 * i + 1][n][1]), cvtpk(facc[2 * i + 1][n][2], facc[2 * i + 1][n][3])};
        *(u32x4*)(pp + (n * 14 + 2 * w + i) * 1024) = v;
      }
  }
}

__global__ __launch_bounds__(256) void reduce_heads(const float* __restrict__ partial, __hip_bfloat16* __restrict__ out) {
  const int idx = blockIdx.x * 256 + threadIdx.x;
  const int lane = idx & 63;
  const int T = (idx >> 6) % 126;
  const int pb = (idx >> 6) / 126;
  if (pb >= 16) return;
  const char* src = (const char*)partial + (size_t)pb * NH * (126 * 1024) + T * 1024 + lane * 16;
  u32x4 v[NH];
#pragma unroll
  for (int hh = 0; hh < NH; ++hh) v[hh] = *(const u32x4*)(src + (size_t)hh * (126 * 1024));
  float s[8] = {0.f, 0.f, 0.f, 0.f, 0.f, 0.f, 0.f, 0.f};
#pragma unroll
  for (int hh = 0; hh < NH; ++hh)
#pragma unroll
    for (int j = 0; j < 4; ++j) {
      s[2 * j] += __uint_as_float(v[hh][j] << 16);
      s[2 * j + 1] += __uint_as_float(v[hh][j] & 0xffff0000u);
    }
  const int n = T / 14, pr = T % 14;
  const int p = pb >> 3, b = pb & 7;
  const int row = 16 * n + (lane & 15), col = 32 * pr + 4 * (lane >> 4);
  char* o = (char*)out + ((size_t)(b * SQ + row) * (2 * DM) + p * DM + col) * 2;
  *(u32x2*)o = (u32x2){cvtpk(s[0], s[1]), cvtpk(s[2], s[3])};
  *(u32x2*)(o + 32) = (u32x2){cvtpk(s[4], s[5]), cvtpk(s[6], s[7])};
}

}

extern "C" void kernel_launch(void* const* d_in, const int* in_sizes, int n_in,
                              void* d_out, int out_size, void* d_ws, size_t ws_size,
                              hipStream_t stream) {
  (void)in_sizes; (void)n_in; (void)out_size; (void)ws_size;
  (void)hipFuncSetAttribute((const void*)fused_attn, hipFuncAttributeMaxDynamicSharedMemorySize, LDS_TOTAL);
  float* partial = (float*)d_ws;
  fused_attn<<<256, 512, LDS_TOTAL, stream>>>(
      (const __hip_bfloat16*)d_in[0], (const __hip_bfloat16*)d_in[1], (const __hip_bfloat16*)d_in[2],
      (const __hip_bfloat16*)d_in[3], (const __hip_bfloat16*)d_in[4],
      (const __hip_bfloat16*)d_in[5], (const __hip_bfloat16*)d_in[6], (const __hip_bfloat16*)d_in[7],
      (const __hip_bfloat16*)d_in[8], (const __hip_bfloat16*)d_in[9], partial);
  reduce_heads<<<(16 * 126 * 64 + 255) / 256, 256, 0, stream>>>(partial, (__hip_bfloat16*)d_out);
}
```

```cpp
#include <hip/hip_runtime.h>
#include <hip/hip_bf16.h>
#include <stdint.h>

namespace {
#define KNT ""
#define VNT " nt"

typedef __attribute__((ext_vector_type(8))) short bf16x8;
typedef __attribute__((ext_vector_type(4))) short s16x4;
typedef __attribute__((ext_vector_type(4))) float f32x4;
typedef __attribute__((ext_vector_type(2))) float f32x2;
typedef __attribute__((ext_vector_type(4))) unsigned u32x4;
typedef __attribute__((ext_vector_type(2))) unsigned u32x2;
typedef __attribute__((ext_vector_type(2))) __bf16 bf16x2_t;
typedef __attribute__((address_space(3))) s16x4* lds_tr_ptr;

constexpr int NB = 8, SQ = 144, DM = 448, NH = 16, SK = 512, DH = 256;
constexpr int NT = 9;

constexpr int LDS_A = 0;
constexpr int LDS_B = 73728;
constexpr int LDS_STAT = 155648;
constexpr int LDS_TOTAL = 163840;
constexpr int QSTAGE = 51200;
constexpr int QX = 18432;

__device__ __forceinline__ void dma16(const void* sbase, unsigned voff, unsigned lds_dst) {
  unsigned keep;
  asm volatile("s_mov_b32 %0, m0\n\ts_mov_b32 m0, %3\n\ts_nop 0\n\tglobal_load_lds_dwordx4 %1, %2\n\ts_mov_b32 m0, %0"
               : "=&s"(keep) : "v"(voff), "s"(sbase), "s"(lds_dst) : "memory");
}
__device__ __forceinline__ bf16x8 gload16(const void* sbase, unsigned voff) {
  bf16x8 r;
  asm volatile("global_load_dwordx4 %0, %1, %2" KNT : "=v"(r) : "v"(voff), "s"(sbase) : "memory");
  return r;
}
#define PIN4(a) asm volatile("" : "+v"(a[0]), "+v"(a[1]), "+v"(a[2]), "+v"(a[3]))
__device__ __forceinline__ void dma16v(const void* sbase, unsigned voff, unsigned lds_dst) {
  unsigned keep;
  asm volatile("s_mov_b32 %0, m0\n\ts_mov_b32 m0, %3\n\ts_nop 0\n\tglobal_load_lds_dwordx4 %1, %2" VNT "\n\ts_mov_b32 m0, %0"
               : "=&s"(keep) : "v"(voff), "s"(sbase), "s"(lds_dst) : "memory");
}
template <int N> __device__ __forceinline__ void wait_vm() { asm volatile("s_waitcnt vmcnt(%0)" ::"i"(N) : "memory"); }
#define WAIT_LGKM0() asm volatile("s_waitcnt lgkmcnt(0)" ::: "memory")
#define SB() __builtin_amdgcn_sched_barrier(0)
__device__ __forceinline__ void bar() { WAIT_LGKM0(); __builtin_amdgcn_s_barrier(); asm volatile("" ::: "memory"); }

__device__ __forceinline__ unsigned cvtpk(float lo, float hi) {
  f32x2 v = {lo, hi};
  bf16x2_t r = __builtin_convertvector(v, bf16x2_t);
  return __builtin_bit_cast(unsigned, r);
}
__device__ __forceinline__ bf16x8 tr2(const char* base, unsigned a0, unsigned a1) {
  s16x4 l = __builtin_amdgcn_ds_read_tr16_b64_v4i16((lds_tr_ptr)(base + a0));
  s16x4 h = __builtin_amdgcn_ds_read_tr16_b64_v4i16((lds_tr_ptr)(base + a1));
  return (bf16x8){l[0], l[1], l[2], l[3], h[0], h[1], h[2], h[3]};
}
__device__ __forceinline__ float xmax(float v) {
  auto a = __builtin_amdgcn_permlane16_swap(__float_as_uint(v), __float_as_uint(v), false, false);
  v = fmaxf(__uint_as_float(a[0]), __uint_as_float(a[1]));
  auto b = __builtin_amdgcn_permlane32_swap(__float_as_uint(v), __float_as_uint(v), false, false);
  return fmaxf(__uint_as_float(b[0]), __uint_as_float(b[1]));
}
__device__ __forceinline__ float xsum(float v) {
  auto a = __builtin_amdgcn_permlane16_swap(__float_as_uint(v), __float_as_uint(v), false, false);
  v = __uint_as_float(a[0]) + __uint_as_float(a[1]);
  auto b = __builtin_amdgcn_permlane32_swap(__float_as_uint(v), __float_as_uint(v), false, false);
  return __uint_as_float(b[0]) + __uint_as_float(b[1]);
}
template <int NM, int NR> __device__ __forceinline__ void ilv() {
#pragma unroll
  for (int i = 0; i < NM; ++i) {
    __builtin_amdgcn_sched_group_barrier(0x008, 1, 0);
    if (i < NR) __builtin_amdgcn_sched_group_barrier(0x100, 1, 0);
  }
}
#define MFMA(a, b, c) __builtin_amdgcn_mfma_f32_16x16x32_bf16(a, b, c, 0, 0, 0)

template <int NA, int NN> struct Frag { bf16x8 a[NA]; bf16x8 b[NN]; };
template <int NA, int NN>
__device__ __forceinline__ void mma(f32x4 (&acc)[NA][NN], const Frag<NA, NN>& f) {
#pragma unroll
  for (int n = 0; n < NN; ++n)
#pragma unroll
    for (int m = 0; m < NA; ++m) acc[m][n] = MFMA(f.a[m], f.b[n], acc[m][n]);
}

#define RELANE() int lane = threadIdx.x & 63; asm volatile("" : "+v"(lane)); \
  const int l15 = lane & 15, g = lane >> 4, q4 = (lane >> 2) & 3, p4 = lane & 3; (void)l15; (void)g; (void)q4; (void)p4;
struct Ctx {
  char* smem;
  int lane, w, l15, g, q4, p4, hi;
  const char *xb, *kh, *vh, *wqh, *woh;
};

constexpr int PSZ(int i) { return i < 3 ? 4 : 2; }
template <int NN, int XCNT, class PRE>
__device__ __forceinline__ void phase_q(const Ctx& c, int nb, PRE pre) {
  char* smem = c.smem;
  const int w = c.w, lane = c.lane, g = c.g, l15 = c.l15, q4 = c.q4, p4 = c.p4, hi = c.hi, cc = c.w & 3;
  const unsigned x_voff = (unsigned)((8 * w + (lane >> 3)) * 896 + (((lane & 7) ^ ((((w & 1) << 2) + g) & 7)) << 4));
  unsigned wq_voff[4];
#pragma unroll
  for (int jj = 0; jj < 4; ++jj) {
    const int r = 2 * jj + hi;
    const int f = (r & 3) | ((w & 1) << 2);
    wq_voff[jj] = (unsigned)((8 * w + r) * 8192 + (((lane & 31) ^ (f << 1)) << 4));
  }
  auto issue = [&](int s) {
    const char* xs = c.xb + s * 128;
    const unsigned dst = (s & 1) * QSTAGE;
    dma16(xs, x_voff, dst + w * 1024);
    dma16(xs + 64 * 896, x_voff, dst + (w + 8) * 1024);
    if (XCNT == 3) dma16(xs + 128 * 896, x_voff, dst + (w + 16) * 1024);
    const char* ws = c.wqh + (size_t)s * 64 * 8192;
#pragma unroll
    for (int jj = 0; jj < 4; ++jj) dma16(ws, wq_voff[jj], dst + QX + (4 * w + jj) * 1024);
  };
  constexpr int CNT = XCNT + 4;
  const int ftr = q4 | ((g & 1) << 2);
  unsigned qa[4];
#pragma unroll
  for (int m = 0; m < 4; ++m) qa[m] = QX + (8 * g + q4) * 512 + (((4 * cc + m) ^ ftr) << 5) + p4 * 8;
  const unsigned xr0 = (nb * 16 + l15) * 128 + ((g ^ (l15 >> 1)) << 4);
  const unsigned xr1 = xr0 ^ 64;
  auto load = [&](Frag<4, NN>& f, int s, int ks) {
    const char* sb = smem + (s & 1) * QSTAGE;
#pragma unroll
    for (int m = 0; m < 4; ++m) f.a[m] = tr2(sb, qa[m] + ks * 16384, qa[m] + ks * 16384 + 2048);
#pragma unroll
    for (int n = 0; n < NN; ++n) f.b[n] = *(const bf16x8*)(sb + (ks ? xr1 : xr0) + n * 2048);
  };
  f32x4 acc[4][NN];
#pragma unroll
  for (int m = 0; m < 4; ++m)
#pragma unroll
    for (int n = 0; n < NN; ++n) acc[m][n] = (f32x4){0.f, 0.f, 0.f, 0.f};
  Frag<4, NN> f0, f1;
  issue(0); issue(1); pre(0);
  wait_vm<CNT + PSZ(0)>();
  bar();
  load(f0, 0, 0);
#pragma unroll
  for (int s = 0; s < 7; ++s) {
    load(f1, s, 1);
    mma<4, NN>(acc, f0);
    ilv<4 * NN, 8 + NN>();
    SB();
    if (s < 6) {
      if (s == 0) wait_vm<PSZ(0)>(); else if (s == 1) wait_vm<PSZ(1)>(); else if (s == 2) wait_vm<PSZ(2)>();
      else if (s == 3) wait_vm<PSZ(3)>(); else if (s == 4) wait_vm<PSZ(4)>(); else wait_vm<PSZ(5)>();
      bar();
      if (s + 2 < 7) { issue(s + 2); pre(s + 1); }
      load(f0, s + 1, 0);
    }
    mma<4, NN>(acc, f1);
    if (s < 6) ilv<4 * NN, 8 + NN>();
    SB();
  }
  bar();
#pragma unroll
  for (int m = 0; m < 4; ++m)
#pragma unroll
    for (int n = 0; n < NN; ++n) {
      u32x2 v = {cvtpk(acc[m][n][0], acc[m][n][1]), cvtpk(acc[m][n][2], acc[m][n][3])};
      const unsigned a = (16 * (nb + n) + l15) * 512 + (((8 * cc + 2 * m + (g >> 1)) ^ l15) << 4) + (g & 1) * 8;
      *(u32x2*)(smem + LDS_A + a) = v;
    }
}

__global__ __launch_bounds__(512, 2) void fused_attn(
    const __hip_bfloat16* __restrict__ xA, const __hip_bfloat16* __restrict__ kA, const __hip_bfloat16* __restrict__ vA,
    const __hip_bfloat16* __restrict__ wqA, const __hip_bfloat16* __restrict__ woA,
    const __hip_bfloat16* __restrict__ xB, const __hip_bfloat16* __restrict__ kB, const __hip_bfloat16* __restrict__ vB,
    const __hip_bfloat16* __restrict__ wqB, const __hip_bfloat16* __restrict__ woB,
    float* __restrict__ partial) {
  extern __shared__ __attribute__((aligned(16))) char smem[];
  const int tid = threadIdx.x;
  Ctx c;
  c.smem = smem;
  c.lane = tid & 63;
  c.w = __builtin_amdgcn_readfirstlane(tid >> 6);
  c.l15 = c.lane & 15; c.g = c.lane >> 4; c.q4 = (c.lane >> 2) & 3; c.p4 = c.lane & 3; c.hi = c.lane >> 5;
  const int w = c.w;

  const int bid = blockIdx.x;
  const int xcd = bid & 7, rr = bid >> 3;
  const int p = xcd >> 2, h = (xcd & 3) * 4 + (rr >> 3), b = rr & 7;

  c.xb = (const char*)((p ? xB : xA) + (size_t)b * SQ * DM);
  c.kh = (const char*)((p ? kB : kA) + (size_t)(b * NH + h) * SK * DH);
  c.vh = (const char*)((p ? vB : vA) + (size_t)(b * NH + h) * SK * DH);
  c.wqh = (const char*)((p ? wqB : wqA) + h * DH);
  c.woh = (const char*)((p ? woB : woA) + (size_t)h * DH * DM);

  unsigned v_voff, k_voff;
  { RELANE(); v_voff = (unsigned)((lane >> 2) * 512 + 64 * w + (((lane & 3) ^ ((g & 1) << 1)) << 4));
    k_voff = (unsigned)((64 * w + l15) * 512 + g * 16); }
  const unsigned vring = LDS_B + w * 2048;
  auto issue_v = [&](int t) {
    const char* src = c.vh + (size_t)(64 * (t & 7) + 32 * (t >> 3)) * 512;
    dma16v(src, v_voff, vring + ((t + 2) % 5) * 16384);
    dma16v(src + 16 * 512, v_voff, vring + ((t + 2) % 5) * 16384 + 1024);
  };
  bf16x8 kf[3][4];
  auto load_k = [&](int ks) {
#pragma unroll
    for (int m = 0; m < 4; ++m) kf[ks % 3][m] = gload16(c.kh + m * 16 * 512 + ks * 64, k_voff);
  };
  auto pre = [&](int i) { if (i < 3) load_k(i); else issue_v(i - 3); };
  if (w < 2) phase_q<5, 3>(c, 0, pre); else if (w < 4) phase_q<5, 2>(c, 0, pre); else phase_q<4, 2>(c, 5, pre);
  issue_v(3); issue_v(4);
  bar();
  f32x4 sacc[4][NT];
#pragma unroll
  for (int m = 0; m < 4; ++m)
#pragma unroll
    for (int n = 0; n < NT; ++n) sacc[m][n] = (f32x4){0.f, 0.f, 0.f, 0.f};
  {
    RELANE();
    const unsigned rowb = LDS_A + l15 * 512;
    bf16x8 bq[2][3];
    auto loadq = [&](bf16x8 (&d)[3], int idx) {
      const int ks = idx / 3, grp = idx % 3;
      const unsigned ca = rowb + (((4 * ks + g) ^ l15) << 4);
#pragma unroll
      for (int j = 0; j < 3; ++j) d[j] = *(const bf16x8*)(smem + ca + (grp * 3 + j) * 8192);
    };
    loadq(bq[0], 0);
#pragma unroll
    for (int idx = 0; idx < 24; ++idx) {
      const int ks = idx / 3, grp = idx % 3;
      if (idx + 1 < 24) loadq(bq[(idx + 1) & 1], idx + 1);
      if (grp == 0) {
        if (ks == 0) wait_vm<4>();
        else if (ks >= 3 && ks < 6) wait_vm<8>(); else if (ks == 6) wait_vm<4>(); else if (ks == 7) wait_vm<0>();
        PIN4(kf[ks % 3]);
      }
      SB();
#pragma unroll
      for (int j = 0; j < 3; ++j)
#pragma unroll
        for (int m = 0; m < 4; ++m) sacc[m][grp * 3 + j] = MFMA(kf[ks % 3][m], bq[idx & 1][j], sacc[m][grp * 3 + j]);
      SB();
      if (grp == 2 && ks + 3 < 8) load_k(ks + 3);
    }
  }
  float* smax = (float*)(smem + LDS_STAT);
  float* ssum = (float*)(smem + LDS_STAT);
  u32x4 pk1[NT];
  {
  RELANE();
#pragma unroll
  for (int n = 0; n < NT; ++n) {
    float m0 = fmaxf(fmaxf(sacc[0][n][0], sacc[0][n][1]), fmaxf(sacc[0][n][2], sacc[0][n][3]));
    float m1 = fmaxf(fmaxf(sacc[1][n][0], sacc[1][n][1]), fmaxf(sacc[1][n][2], sacc[1][n][3]));
    float m2 = fmaxf(fmaxf(sacc[2][n][0], sacc[2][n][1]), fmaxf(sacc[2][n][2], sacc[2][n][3]));
    float m3 = fmaxf(fmaxf(sacc[3][n][0], sacc[3][n][1]), fmaxf(sacc[3][n][2], sacc[3][n][3]));
    const float m = xmax(fmaxf(fmaxf(m0, m1), fmaxf(m2, m3)));
    smax[(16 * n + l15) * 8 + w] = m;
  }
  bar();
  constexpr float CEXP = 0.0625f * 1.4426950408889634f;
  float mc[NT];
#pragma unroll
  for (int n = 0; n < NT; ++n) {
    const f32x4 m0 = *(const f32x4*)(smax + (16 * n + l15) * 8);
    const f32x4 m1 = *(const f32x4*)(smax + (16 * n + l15) * 8 + 4);
    mc[n] = -CEXP * fmaxf(fmaxf(fmaxf(m0[0], m0[1]), fmaxf(m0[2], m0[3])), fmaxf(fmaxf(m1[0], m1[1]), fmaxf(m1[2], m1[3])));
  }
  float rsum[NT];
  const unsigned pw = LDS_A + l15 * 512 + (((4 * w + g) ^ l15) << 4);
#pragma unroll
  for (int n = 0; n < NT; ++n) {
    float e[4][4];
#pragma unroll
    for (int mm = 0; mm < 4; ++mm)
#pragma unroll
      for (int r = 0; r < 4; ++r) e[mm][r] = __builtin_amdgcn_exp2f(fmaf(sacc[mm][n][r], CEXP, mc[n]));
    const float s0 = (e[0][0] + e[0][1]) + (e[0][2] + e[0][3]), s1 = (e[1][0] + e[1][1]) + (e[1][2] + e[1][3]);
    const float s2 = (e[2][0] + e[2][1]) + (e[2][2] + e[2][3]), s3 = (e[3][0] + e[3][1]) + (e[3][2] + e[3][3]);
    rsum[n] = xsum((s0 + s1) + (s2 + s3));
    u32x4 p0 = {cvtpk(e[0][0], e[0][1]), cvtpk(e[0][2], e[0][3]), cvtpk(e[1][0], e[1][1]), cvtpk(e[1][2], e[1][3])};
    *(u32x4*)(smem + pw + n * 8192) = p0;
    pk1[n] = (u32x4){cvtpk(e[2][0], e[2][1]), cvtpk(e[2][2], e[2][3]), cvtpk(e[3][0], e[3][1]), cvtpk(e[3][2], e[3][3])};
  }
  bar();
#pragma unroll
  for (int n = 0; n < NT; ++n) ssum[(16 * n + l15) * 8 + w] = rsum[n];
  }
  f32x4 oacc[2][NT];
#pragma unroll
  for (int m = 0; m < 2; ++m)
#pragma unroll
    for (int n = 0; n < NT; ++n) oacc[m][n] = (f32x4){0.f, 0.f, 0.f, 0.f};
  {
    RELANE();
    const unsigned pw = LDS_A + l15 * 512 + (((4 * w + g) ^ l15) << 4);
    const unsigned va0 = vring + (4 * g + q4) * 64 + ((0 ^ (g & 1)) << 5) + p4 * 8;
    const unsigned va1 = vring + (4 * g + q4) * 64 + ((1 ^ (g & 1)) << 5) + p4 * 8;
    const unsigned rowb = LDS_A + l15 * 512;
    auto load = [&](Frag<2, NT>& f, int t) {
      const char* vs = smem + ((t + 2) % 5) * 16384;
      f.a[0] = tr2(vs, va0, va0 + 1024);
      f.a[1] = tr2(vs, va1, va1 + 1024);
      const unsigned ca = rowb + (((4 * (t & 7) + g) ^ l15) << 4);
#pragma unroll
      for (int n = 0; n < NT; ++n) f.b[n] = *(const bf16x8*)(smem + ca + n * 8192);
    };
    Frag<2, NT> f0, f1;
    wait_vm<8>();
    load(f0, 0);
    auto step = [&](Frag<2, NT>& cur, Frag<2, NT>& nxt, int t) {
      if (t < 15) {
        WAIT_LGKM0();
        if (t + 5 < 16) issue_v(t + 5);
        if (t + 5 < 16) wait_vm<8>(); else if (t == 11) wait_vm<6>(); else if (t == 12) wait_vm<4>(); else if (t == 13) wait_vm<2>(); else wait_vm<0>();
        if (t == 7) {
          bar();
#pragma unroll
          for (int n = 0; n < NT; ++n) *(u32x4*)(smem + pw + n * 8192) = pk1[n];
          bar();
        }
        load(nxt, t + 1);
      }
      mma<2, NT>(oacc, cur);
      if (t < 15) ilv<18, 13>();
      SB();
    };
#pragma unroll
    for (int t = 0; t < 16; t += 2) { step(f0, f1, t); step(f1, f0, t + 1); }
  }
  RELANE();
  const unsigned pw = LDS_A + l15 * 512 + (((4 * w + g) ^ l15) << 4);
  float rinv[NT];
#pragma unroll
  for (int n = 0; n < NT; ++n) {
    const f32x4 s0 = *(const f32x4*)(ssum + (16 * n + l15) * 8);
    const f32x4 s1 = *(const f32x4*)(ssum + (16 * n + l15) * 8 + 4);
    rinv[n] = 1.0f / (((s0[0] + s0[1]) + (s0[2] + s0[3])) + ((s1[0] + s1[1]) + (s1[2] + s1[3])));
  }
  bar();

  const unsigned wo_voff = (unsigned)((lane >> 3) * 896 + 128 * w + (((lane & 7) ^ (g << 1)) << 4));
  const unsigned woring = LDS_B + w * 12288;
  auto issue_wo = [&](int ks) {
    const char* src = c.woh + (size_t)ks * 32 * 896;
    const unsigned dst = woring + (ks % 3) * 4096;
#pragma unroll
    for (int j = 0; j < 4; ++j) dma16(src + j * 8 * 896, wo_voff, dst + j * 1024);
  };
  if (w < 7) { issue_wo(0); issue_wo(1); issue_wo(2); }
#pragma unroll
  for (int n = 0; n < NT; ++n) {
    const float s = rinv[n];
    u32x4 v = {cvtpk(oacc[0][n][0] * s, oacc[0][n][1] * s), cvtpk(oacc[0][n][2] * s, oacc[0][n][3] * s),
               cvtpk(oacc[1][n][0] * s, oacc[1][n][1] * s), cvtpk(oacc[1][n][2] * s, oacc[1][n][3] * s)};
    *(u32x4*)(smem + pw + n * 8192) = v;
  }
  bar();
  if (w < 7) {
    const int swo = (2 * g + (q4 >> 1)) & 3;
    unsigned oa[4];
#pragma unroll
    for (int i = 0; i < 4; ++i) oa[i] = woring + (4 * g + q4) * 128 + ((i ^ swo) << 5) + p4 * 8;
    const unsigned rowb = LDS_A + l15 * 512;
    f32x4 facc[4][NT];
#pragma unroll
    for (int i = 0; i < 4; ++i)
#pragma unroll
      for (int n = 0; n < NT; ++n) facc[i][n] = (f32x4){0.f, 0.f, 0.f, 0.f};
    bf16x8 af[2][4], bo[2][3];
    auto loada = [&](bf16x8 (&d)[4], int ks) {
#pragma unroll
      for (int i = 0; i < 4; ++i) d[i] = tr2(smem + (ks % 3) * 4096, oa[i], oa[i] + 2048);
    };
    auto loadb = [&](bf16x8 (&d)[3], int idx) {
      const int ks = idx / 3, grp = idx % 3;
      const unsigned ca = rowb + (((4 * ks + g) ^ l15) << 4);
#pragma unroll
      for (int j = 0; j < 3; ++j) d[j] = *(const bf16x8*)(smem + ca + (grp * 3 + j) * 8192);
    };
    wait_vm<8>();
    loada(af[0], 0);
    loadb(bo[0], 0);
#pragma unroll
    for (int idx = 0; idx < 24; ++idx) {
      const int ks = idx / 3, grp = idx % 3;
      if (grp == 0 && ks > 0 && ks + 2 < 8) { WAIT_LGKM0(); issue_wo(ks + 2); }
      if (grp == 1 && ks < 7) {
        if (ks < 6) wait_vm<4>(); else wait_vm<0>();
        loada(af[(ks + 1) & 1], ks + 1);
      }
      if (idx + 1 < 24) loadb(bo[(idx + 1) & 1], idx + 1);
      SB();
#pragma unroll
      for (int j = 0; j < 3; ++j)
#pragma unroll
        for (int i = 0; i < 4; ++i) facc[i][grp * 3 + j] = MFMA(af[ks & 1][i], bo[idx & 1][j], facc[i][grp * 3 + j]);
      SB();
    }
    char* pp = (char*)partial + (size_t)((p * NB + b) * NH + h) * (126 * 1024) + lane * 16;
#pragma unroll
    for (int i = 0; i < 2; ++i)
#pragma unroll
      for (int n = 0; n < NT; ++n) {
        u32x4 v = {cvtpk(facc[2 * i][n][0], facc[2 * i][n][1]), cvtpk(facc[2 * i][n][2], facc[2 * i][n][3]),
                   cvtpk(facc[2 * i + 1][n][0], facc[2 * i + 1][n][1]), cvtpk(facc[2 * i + 1][n][2], facc[2 * i + 1][n][3])};
        *(u32x4*)(pp + (n * 14 + 2 * w + i) * 1024) = v;
      }
  }
}

__global__ __launch_bounds__(256) void reduce_heads(const float* __restrict__ partial, __hip_bfloat16* __restrict__ out) {
  const int idx = blockIdx.x * 256 + threadIdx.x;
  const int lane = idx & 63;
  const int T = (idx >> 6) % 126;
  const int pb = (idx >> 6) / 126;
  if (pb >= 16) return;
  const char* src = (const char*)partial + (size_t)pb * NH * (126 * 1024) + T * 1024 + lane * 16;
  u32x4 v[NH];
#pragma unroll
  for (int hh = 0; hh < NH; ++hh) v[hh] = *(const u32x4*)(src + (size_t)hh * (126 * 1024));
  float s[8] = {0.f, 0.f, 0.f, 0.f, 0.f, 0.f, 0.f, 0.f};
#pragma unroll
  for (int hh = 0; hh < NH; ++hh)
#pragma unroll
    for (int j = 0; j < 4; ++j) {
      s[2 * j] += __uint_as_float(v[hh][j] << 16);
      s[2 * j + 1] += __uint_as_float(v[hh][j] & 0xffff0000u);
    }
  const int n = T / 14, pr = T % 14;
  const int p = pb >> 3, b = pb & 7;
  const int row = 16 * n + (lane & 15), col = 32 * pr + 4 * (lane >> 4);
  char* o = (char*)out + ((size_t)(b * SQ + row) * (2 * DM) + p * DM + col) * 2;
  *(u32x2*)o = (u32x2){cvtpk(s[0], s[1]), cvtpk(s[2], s[3])};
  *(u32x2*)(o + 32) = (u32x2){cvtpk(s[4], s[5]), cvtpk(s[6], s[7])};
}

}

extern "C" void kernel_launch(void* const* d_in, const int* in_sizes, int n_in,
                              void* d_out, int out_size, void* d_ws, size_t ws_size,
                              hipStream_t stream) {
  (void)in_sizes; (void)n_in; (void)out_size; (void)ws_size;
  (void)hipFuncSetAttribute((const void*)fused_attn, hipFuncAttributeMaxDynamicSharedMemorySize, LDS_TOTAL);
  float* partial = (float*)d_ws;
  fused_attn<<<256, 512, LDS_TOTAL, stream>>>(
      (const __hip_bfloat16*)d_in[0], (const __hip_bfloat16*)d_in[1], (const __hip_bfloat16*)d_in[2],
      (const __hip_bfloat16*)d_in[3], (const __hip_bfloat16*)d_in[4],
      (const __hip_bfloat16*)d_in[5], (const __hip_bfloat16*)d_in[6], (const __hip_bfloat16*)d_in[7],
      (const __hip_bfloat16*)d_in[8], (const __hip_bfloat16*)d_in[9], partial);
  reduce_heads<<<(16 * 126 * 64 + 255) / 256, 256, 0, stream>>>(partial, (__hip_bfloat16*)d_out);
}
```

```cpp
#include <hip/hip_runtime.h>
#include <hip/hip_bf16.h>
#include <stdint.h>


namespace {

typedef __attribute__((ext_vector_type(8))) short bf16x8;
typedef __attribute__((ext_vector_type(4))) short s16x4;
typedef __attribute__((ext_vector_type(4))) float f32x4;
typedef __attribute__((ext_vector_type(2))) float f32x2;
typedef __attribute__((ext_vector_type(4))) unsigned u32x4;
typedef __attribute__((ext_vector_type(2))) unsigned u32x2;
typedef __attribute__((ext_vector_type(2))) __bf16 bf16x2_t;
typedef __attribute__((address_space(3))) s16x4* lds_tr_ptr;

constexpr int NB = 8, SQ = 144, DM = 448, NH = 16, SK = 512, DH = 256;
constexpr int NT = 9;

constexpr int LDS_A = 0;
constexpr int LDS_B = 73728;
constexpr int LDS_STAT = 155648;
constexpr int LDS_TOTAL = 163840;
constexpr int QSTAGE = 51200;
constexpr int QX = 18432;

__device__ __forceinline__ void dma16(const void* sbase, unsigned voff, unsigned lds_dst) {
  unsigned keep;
  asm volatile("s_mov_b32 %0, m0\n\ts_mov_b32 m0, %3\n\ts_nop 0\n\tglobal_load_lds_dwordx4 %1, %2\n\ts_mov_b32 m0, %0"
               : "=&s"(keep) : "v"(voff), "s"(sbase), "s"(lds_dst) : "memory");
}
__device__ __forceinline__ bf16x8 gload16(const void* sbase, unsigned voff) {
  bf16x8 r;
  asm volatile("global_load_dwordx4 %0, %1, %2" : "=v"(r) : "v"(voff), "s"(sbase) : "memory");
  return r;
}
#define PIN4(a) asm volatile("" : "+v"(a[0]), "+v"(a[1]), "+v"(a[2]), "+v"(a[3]))
template <int N> __device__ __forceinline__ void wait_vm() { asm volatile("s_waitcnt vmcnt(%0)" ::"i"(N) : "memory"); }
#define WAIT_LGKM0() asm volatile("s_waitcnt lgkmcnt(0)" ::: "memory")
#define SB() __builtin_amdgcn_sched_barrier(0)
__device__ __forceinline__ void bar() { WAIT_LGKM0(); __builtin_amdgcn_s_barrier(); asm volatile("" ::: "memory"); }

__device__ __forceinline__ unsigned cvtpk(float lo, float hi) {
  f32x2 v = {lo, hi};
  bf16x2_t r = __builtin_convertvector(v, bf16x2_t);
  return __builtin_bit_cast(unsigned, r);
}
__device__ __forceinline__ bf16x8 tr2(const char* base, unsigned a0, unsigned a1) {
  s16x4 l = __builtin_amdgcn_ds_read_tr16_b64_v4i16((lds_tr_ptr)(base + a0));
  s16x4 h = __builtin_amdgcn_ds_read_tr16_b64_v4i16((lds_tr_ptr)(base + a1));
  return (bf16x8){l[0], l[1], l[2], l[3], h[0], h[1], h[2], h[3]};
}
__device__ __forceinline__ float xmax(float v) {
  auto a = __builtin_amdgcn_permlane16_swap(__float_as_uint(v), __float_as_uint(v), false, false);
  v = fmaxf(__uint_as_float(a[0]), __uint_as_float(a[1]));
  auto b = __builtin_amdgcn_permlane32_swap(__float_as_uint(v), __float_as_uint(v), false, false);
  return fmaxf(__uint_as_float(b[0]), __uint_as_float(b[1]));
}
__device__ __forceinline__ float xsum(float v) {
  auto a = __builtin_amdgcn_permlane16_swap(__float_as_uint(v), __float_as_uint(v), false, false);
  v = __uint_as_float(a[0]) + __uint_as_float(a[1]);
  auto b = __builtin_amdgcn_permlane32_swap(__float_as_uint(v), __float_as_uint(v), false, false);
  return __uint_as_float(b[0]) + __uint_as_float(b[1]);
}
template <int NM, int NR> __device__ __forceinline__ void ilv() {
#pragma unroll
  for (int i = 0; i < NM; ++i) {
    __builtin_amdgcn_sched_group_barrier(0x008, 1, 0);
    if (i < NR) __builtin_amdgcn_sched_group_barrier(0x100, 1, 0);
  }
}
#define MFMA(a, b, c) __builtin_amdgcn_mfma_f32_16x16x32_bf16(a, b, c, 0, 0, 0)

template <int NA, int NN> struct Frag { bf16x8 a[NA]; bf16x8 b[NN]; };
template <int NA, int NN>
__device__ __forceinline__ void mma(f32x4 (&acc)[NA][NN], const Frag<NA, NN>& f) {
#pragma unroll
  for (int n = 0; n < NN; ++n)
#pragma unroll
    for (int m = 0; m < NA; ++m) acc[m][n] = MFMA(f.a[m], f.b[n], acc[m][n]);
}

#define RELANE() int lane = threadIdx.x & 63; asm volatile("" : "+v"(lane)); \
  const int l15 = lane & 15, g = lane >> 4, q4 = (lane >> 2) & 3, p4 = lane & 3; (void)l15; (void)g; (void)q4; (void)p4;
struct Ctx {
  char* smem;
  int lane, w, l15, g, q4, p4, hi;
  const char *xb, *kh, *vh, *wqh, *woh;
};

template <int NN, int XCNT, class KPRE>
__device__ __forceinline__ void phase_q(const Ctx& c, int nb, KPRE kpre) {
  char* smem = c.smem;
  const int w = c.w, lane = c.lane, g = c.g, l15 = c.l15, q4 = c.q4, p4 = c.p4, hi = c.hi, cc = c.w & 3;
  unsigned x_voff = 0, wq_voff[8];
  if (XCNT) {
    x_voff = (unsigned)((8 * w + (lane >> 3)) * 896 + (((lane & 7) ^ ((((w & 1) << 2) + g) & 7)) << 4));
#pragma unroll
    for (int jj = 0; jj < 8; ++jj) {
      const int r = 2 * jj + hi;
      const int f = (r & 3) | (((r >> 3) & 1) << 2);
      wq_voff[jj] = (unsigned)((16 * w + r) * 8192 + (((lane & 31) ^ (f << 1)) << 4));
    }
  }
  auto issue = [&](int s) {
    const char* xs = c.xb + s * 128;
    const unsigned dst = (s & 1) * QSTAGE;
#pragma unroll
    for (int i = 0; i < XCNT; ++i) dma16(xs + i * 32 * 896, x_voff, dst + (w + 4 * i) * 1024);
    const char* ws = c.wqh + (size_t)s * 64 * 8192;
#pragma unroll
    for (int jj = 0; jj < 8; ++jj) dma16(ws, wq_voff[jj], dst + QX + (8 * w + jj) * 1024);
  };
  constexpr int CNT = XCNT + 8;
  const int ftr = q4 | ((g & 1) << 2);
  unsigned qa[4];
#pragma unroll
  for (int m = 0; m < 4; ++m) qa[m] = QX + (8 * g + q4) * 512 + (((4 * cc + m) ^ ftr) << 5) + p4 * 8;
  const unsigned xr0 = (nb * 16 + l15) * 128 + ((g ^ (l15 >> 1)) << 4);
  const unsigned xr1 = xr0 ^ 64;
  auto load = [&](Frag<4, NN>& f, int s, int ks) {
    const char* sb = smem + (s & 1) * QSTAGE;
#pragma unroll
    for (int m = 0; m < 4; ++m) f.a[m] = tr2(sb, qa[m] + ks * 16384, qa[m] + ks * 16384 + 2048);
#pragma unroll
    for (int n = 0; n < NN; ++n) f.b[n] = *(const bf16x8*)(sb + (ks ? xr1 : xr0) + n * 2048);
  };
  f32x4 acc[4][NN];
#pragma unroll
  for (int m = 0; m < 4; ++m)
#pragma unroll
    for (int n = 0; n < NN; ++n) acc[m][n] = (f32x4){0.f, 0.f, 0.f, 0.f};
  Frag<4, NN> f0, f1;
  if (XCNT) { issue(0); issue(1); wait_vm<CNT>(); } else kpre();
  bar();
  load(f0, 0, 0);
#pragma unroll
  for (int s = 0; s < 7; ++s) {
    load(f1, s, 1);
    mma<4, NN>(acc, f0);
    ilv<4 * NN, 8 + NN>();
    SB();
    if (s < 6) {
      if (XCNT) { if (s < 5) wait_vm<0>(); else wait_vm<16>(); }
      bar();
      if (XCNT && s + 2 < 7) { issue(s + 2); if (s == 4) kpre(); }
      load(f0, s + 1, 0);
    }
    mma<4, NN>(acc, f1);
    if (s < 6) ilv<4 * NN, 8 + NN>();
    SB();
  }
  bar();
#pragma unroll
  for (int m = 0; m < 4; ++m)
#pragma unroll
    for (int n = 0; n < NN; ++n) {
      u32x2 v = {cvtpk(acc[m][n][0], acc[m][n][1]), cvtpk(acc[m][n][2], acc[m][n][3])};
      const unsigned a = (16 * (nb + n) + l15) * 512 + (((8 * cc + 2 * m + (g >> 1)) ^ l15) << 4) + (g & 1) * 8;
      *(u32x2*)(smem + LDS_A + a) = v;
    }
}

__global__ __launch_bounds__(512, 2) void fused_attn(
    const __hip_bfloat16* __restrict__ xA, const __hip_bfloat16* __restrict__ kA, const __hip_bfloat16* __restrict__ vA,
    const __hip_bfloat16* __restrict__ wqA, const __hip_bfloat16* __restrict__ woA,
    const __hip_bfloat16* __restrict__ xB, const __hip_bfloat16* __restrict__ kB, const __hip_bfloat16* __restrict__ vB,
    const __hip_bfloat16* __restrict__ wqB, const __hip_bfloat16* __restrict__ woB,
    float* __restrict__ partial) {
  extern __shared__ __attribute__((aligned(16))) char smem[];
  const int tid = threadIdx.x;
  Ctx c;
  c.smem = smem;
  c.lane = tid & 63;
  c.w = __builtin_amdgcn_readfirstlane(tid >> 6);
  c.l15 = c.lane & 15; c.g = c.lane >> 4; c.q4 = (c.lane >> 2) & 3; c.p4 = c.lane & 3; c.hi = c.lane >> 5;
  const int w = c.w;

  const int bid = blockIdx.x;
  const int xcd = bid & 7, rr = bid >> 3;
  const int p = xcd >> 2, h = (xcd & 3) * 4 + (rr >> 3), b = rr & 7;

  c.xb = (const char*)((p ? xB : xA) + (size_t)b * SQ * DM);
  c.kh = (const char*)((p ? kB : kA) + (size_t)(b * NH + h) * SK * DH);
  c.vh = (const char*)((p ? vB : vA) + (size_t)(b * NH + h) * SK * DH);
  c.wqh = (const char*)((p ? wqB : wqA) + h * DH);
  c.woh = (const char*)((p ? woB : woA) + (size_t)h * DH * DM);

  unsigned v_voff, k_voff;
  { RELANE(); v_voff = (unsigned)((lane >> 2) * 512 + 64 * w + (((lane & 3) ^ ((g & 1) << 1)) << 4));
    k_voff = (unsigned)((64 * w + l15) * 512 + g * 16); }
  const unsigned vring = LDS_B + w * 2048;
  auto vsrc = [&](int t) { return c.vh + (size_t)(64 * (t & 7) + 32 * (t >> 3)) * 512; };
  auto issue_v = [&](int t, int slot) {
    dma16(vsrc(t), v_voff, vring + slot * 16384);
    dma16(vsrc(t) + 16 * 512, v_voff, vring + slot * 16384 + 1024);
  };
  bf16x8 kf[4][4];
  auto load_k = [&](int ks) {
#pragma unroll
    for (int m = 0; m < 4; ++m) kf[ks & 3][m] = gload16(c.kh + m * 16 * 512 + ks * 64, k_voff);
  };
  auto kpre = [&]() { load_k(0); load_k(1); load_k(2); load_k(3); };
  if (w < 2) phase_q<5, 5>(c, 0, kpre); else if (w < 4) phase_q<5, 4>(c, 0, kpre); else phase_q<4, 0>(c, 5, kpre);
  bar();
  f32x4 sacc[4][NT];
#pragma unroll
  for (int m = 0; m < 4; ++m)
#pragma unroll
    for (int n = 0; n < NT; ++n) sacc[m][n] = (f32x4){0.f, 0.f, 0.f, 0.f};
  {
    RELANE();
    const unsigned rowb = LDS_A + l15 * 512;
    bf16x8 bq[2][3];
    auto loadq = [&](bf16x8 (&d)[3], int idx) {
      const int ks = idx / 3, grp = idx % 3;
      const unsigned ca = rowb + (((4 * ks + g) ^ l15) << 4);
#pragma unroll
      for (int j = 0; j < 3; ++j) d[j] = *(const bf16x8*)(smem + ca + (grp * 3 + j) * 8192);
    };
    loadq(bq[0], 0);
#pragma unroll
    for (int idx = 0; idx < 24; ++idx) {
      const int ks = idx / 3, grp = idx % 3;
      if (idx + 1 < 24) loadq(bq[(idx + 1) & 1], idx + 1);
      if (grp == 0) {
        if (ks < 4) wait_vm<12>(); else if (ks == 4) wait_vm<22>(); else if (ks == 5) wait_vm<18>();
        else if (ks == 6) wait_vm<14>(); else wait_vm<10>();
        PIN4(kf[ks & 3]);
      }
      SB();
#pragma unroll
      for (int j = 0; j < 3; ++j)
#pragma unroll
        for (int m = 0; m < 4; ++m) sacc[m][grp * 3 + j] = MFMA(kf[ks & 3][m], bq[idx & 1][j], sacc[m][grp * 3 + j]);
      if (grp == 2 && ks < 4) load_k(ks + 4);
      if (grp == 2 && ks == 3) { issue_v(0, 0); issue_v(1, 1); issue_v(2, 2); issue_v(3, 3); issue_v(4, 4); }
    }
  }
  bf16x8 vr[8][2];
#pragma unroll
  for (int t = 0; t < 8; ++t) { vr[t][0] = gload16(vsrc(t + 5), v_voff); vr[t][1] = gload16(vsrc(t + 5) + 16 * 512, v_voff); }
  float* smax = (float*)(smem + LDS_STAT);
  float* ssum = (float*)(smem + LDS_STAT);
  u32x4 pk1[NT];
  {
  RELANE();
#pragma unroll
  for (int n = 0; n < NT; ++n) {
    float m0 = fmaxf(fmaxf(sacc[0][n][0], sacc[0][n][1]), fmaxf(sacc[0][n][2], sacc[0][n][3]));
    float m1 = fmaxf(fmaxf(sacc[1][n][0], sacc[1][n][1]), fmaxf(sacc[1][n][2], sacc[1][n][3]));
    float m2 = fmaxf(fmaxf(sacc[2][n][0], sacc[2][n][1]), fmaxf(sacc[2][n][2], sacc[2][n][3]));
    float m3 = fmaxf(fmaxf(sacc[3][n][0], sacc[3][n][1]), fmaxf(sacc[3][n][2], sacc[3][n][3]));
    const float m = xmax(fmaxf(fmaxf(m0, m1), fmaxf(m2, m3)));
    smax[(16 * n + l15) * 8 + w] = m;
  }
  bar();
  constexpr float CEXP = 0.0625f * 1.4426950408889634f;
  float mc[NT];
#pragma unroll
  for (int n = 0; n < NT; ++n) {
    const f32x4 m0 = *(const f32x4*)(smax + (16 * n + l15) * 8);
    const f32x4 m1 = *(const f32x4*)(smax + (16 * n + l15) * 8 + 4);
    mc[n] = -CEXP * fmaxf(fmaxf(fmaxf(m0[0], m0[1]), fmaxf(m0[2], m0[3])), fmaxf(fmaxf(m1[0], m1[1]), fmaxf(m1[2], m1[3])));
  }
  float rsum[NT];
  const unsigned pw = LDS_A + l15 * 512 + (((4 * w + g) ^ l15) << 4);
#pragma unroll
  for (int n = 0; n < NT; ++n) {
    float e[4][4];
#pragma unroll
    for (int mm = 0; mm < 4; ++mm)
#pragma unroll
      for (int r = 0; r < 4; ++r) e[mm][r] = __builtin_amdgcn_exp2f(fmaf(sacc[mm][n][r], CEXP, mc[n]));
    const float s0 = (e[0][0] + e[0][1]) + (e[0][2] + e[0][3]), s1 = (e[1][0] + e[1][1]) + (e[1][2] + e[1][3]);
    const float s2 = (e[2][0] + e[2][1]) + (e[2][2] + e[2][3]), s3 = (e[3][0] + e[3][1]) + (e[3][2] + e[3][3]);
    rsum[n] = xsum((s0 + s1) + (s2 + s3));
    u32x4 p0 = {cvtpk(e[0][0], e[0][1]), cvtpk(e[0][2], e[0][3]), cvtpk(e[1][0], e[1][1]), cvtpk(e[1][2], e[1][3])};
    *(u32x4*)(smem + pw + n * 8192) = p0;
    pk1[n] = (u32x4){cvtpk(e[2][0], e[2][1]), cvtpk(e[2][2], e[2][3]), cvtpk(e[3][0], e[3][1]), cvtpk(e[3][2], e[3][3])};
  }
  bar();
#pragma unroll
  for (int n = 0; n < NT; ++n) ssum[(16 * n + l15) * 8 + w] = rsum[n];
  }
  f32x4 oacc[2][NT];
#pragma unroll
  for (int m = 0; m < 2; ++m)
#pragma unroll
    for (int n = 0; n < NT; ++n) oacc[m][n] = (f32x4){0.f, 0.f, 0.f, 0.f};
  {
    RELANE();
    const unsigned pw = LDS_A + l15 * 512 + (((4 * w + g) ^ l15) << 4);
    const unsigned va0 = vring + (4 * g + q4) * 64 + ((0 ^ (g & 1)) << 5) + p4 * 8;
    const unsigned va1 = vring + (4 * g + q4) * 64 + ((1 ^ (g & 1)) << 5) + p4 * 8;
    const unsigned rowb = LDS_A + l15 * 512;
    auto slot_of = [](int t) { return t < 5 ? t : (t < 13 ? 3 + ((t - 5) & 1) : t - 13); };
    bf16x8 af[2][2], bp[2][3];
    auto loada = [&](bf16x8 (&d)[2], int t) {
      const char* vs = smem + slot_of(t) * 16384;
      d[0] = tr2(vs, va0, va0 + 1024);
      d[1] = tr2(vs, va1, va1 + 1024);
    };
    auto loadb = [&](bf16x8 (&d)[3], int idx) {
      const int t = idx / 3, grp = idx % 3;
      const unsigned ca = rowb + (((4 * (t & 7) + g) ^ l15) << 4);
#pragma unroll
      for (int j = 0; j < 3; ++j) d[j] = *(const bf16x8*)(smem + ca + (grp * 3 + j) * 8192);
    };
    wait_vm<24>();
    loada(af[0], 0);
    loadb(bp[0], 0);
#pragma unroll
    for (int idx = 0; idx < 48; ++idx) {
      const int t = idx / 3, grp = idx % 3;
      if (grp == 0 && t < 15) {
        if (t >= 1 && t <= 3) { WAIT_LGKM0(); issue_v(t + 12, t - 1); }
        if (t <= 3) wait_vm<22>(); else if (t == 4) wait_vm<20>(); else if (t == 5) wait_vm<18>(); else if (t == 6) wait_vm<16>();
        else if (t == 7) wait_vm<14>(); else if (t == 8) wait_vm<12>(); else if (t == 9) wait_vm<10>(); else if (t == 10) wait_vm<8>();
        else if (t == 11) wait_vm<6>(); else if (t == 12) wait_vm<4>(); else if (t == 13) wait_vm<2>(); else wait_vm<0>();
        if (t + 1 >= 5 && t + 1 < 13) {
          asm volatile("" : "+v"(vr[t - 4][0]), "+v"(vr[t - 4][1]));
          char* dst = smem + vring + slot_of(t + 1) * 16384 + lane * 16;
          *(bf16x8*)dst = vr[t - 4][0];
          *(bf16x8*)(dst + 1024) = vr[t - 4][1];
        }
        loada(af[(t + 1) & 1], t + 1);
      }
      if (idx == 23) {
        bar();
#pragma unroll
        for (int n = 0; n < NT; ++n) *(u32x4*)(smem + pw + n * 8192) = pk1[n];
        bar();
      }
      if (idx + 1 < 48) loadb(bp[(idx + 1) & 1], idx + 1);
      SB();
#pragma unroll
      for (int j = 0; j < 3; ++j) {
        oacc[0][grp * 3 + j] = MFMA(af[t & 1][0], bp[idx & 1][j], oacc[0][grp * 3 + j]);
        oacc[1][grp * 3 + j] = MFMA(af[t & 1][1], bp[idx & 1][j], oacc[1][grp * 3 + j]);
      }
      SB();
    }
  }
  RELANE();
  const unsigned pw = LDS_A + l15 * 512 + (((4 * w + g) ^ l15) << 4);
  float rinv[NT];
#pragma unroll
  for (int n = 0; n < NT; ++n) {
    const f32x4 s0 = *(const f32x4*)(ssum + (16 * n + l15) * 8);
    const f32x4 s1 = *(const f32x4*)(ssum + (16 * n + l15) * 8 + 4);
    rinv[n] = 1.0f / (((s0[0] + s0[1]) + (s0[2] + s0[3])) + ((s1[0] + s1[1]) + (s1[2] + s1[3])));
  }
  bar();

  const unsigned wo_voff = (unsigned)((lane >> 3) * 896 + 128 * w + (((lane & 7) ^ (g << 1)) << 4));
  const unsigned woring = LDS_B + w * 12288;
  auto issue_wo = [&](int ks) {
    const char* src = c.woh + (size_t)ks * 32 * 896;
    const unsigned dst = woring + (ks % 3) * 4096;
#pragma unroll
    for (int j = 0; j < 4; ++j) dma16(src + j * 8 * 896, wo_voff, dst + j * 1024);
  };
  if (w < 7) { issue_wo(0); issue_wo(1); issue_wo(2); }
#pragma unroll
  for (int n = 0; n < NT; ++n) {
    const float s = rinv[n];
    u32x4 v = {cvtpk(oacc[0][n][0] * s, oacc[0][n][1] * s), cvtpk(oacc[0][n][2] * s, oacc[0][n][3] * s),
               cvtpk(oacc[1][n][0] * s, oacc[1][n][1] * s), cvtpk(oacc[1][n][2] * s, oacc[1][n][3] * s)};
    *(u32x4*)(smem + pw + n * 8192) = v;
  }
  bar();
  if (w < 7) {
    const int swo = (2 * g + (q4 >> 1)) & 3;
    unsigned oa[4];
#pragma unroll
    for (int i = 0; i < 4; ++i) oa[i] = woring + (4 * g + q4) * 128 + ((i ^ swo) << 5) + p4 * 8;
    const unsigned rowb = LDS_A + l15 * 512;
    f32x4 facc[4][NT];
#pragma unroll
    for (int i = 0; i < 4; ++i)
#pragma unroll
      for (int n = 0; n < NT; ++n) facc[i][n] = (f32x4){0.f, 0.f, 0.f, 0.f};
    bf16x8 af[2][4], bo[2][3];
    auto loada = [&](bf16x8 (&d)[4], int ks) {
#pragma unroll
      for (int i = 0; i < 4; ++i) d[i] = tr2(smem + (ks % 3) * 4096, oa[i], oa[i] + 2048);
    };
    auto loadb = [&](bf16x8 (&d)[3], int idx) {
      const int ks = idx / 3, grp = idx % 3;
      const unsigned ca = rowb + (((4 * ks + g) ^ l15) << 4);
#pragma unroll
      for (int j = 0; j < 3; ++j) d[j] = *(const bf16x8*)(smem + ca + (grp * 3 + j) * 8192);
    };
    wait_vm<8>();
    loada(af[0], 0);
    loadb(bo[0], 0);
#pragma unroll
    for (int idx = 0; idx < 24; ++idx) {
      const int ks = idx / 3, grp = idx % 3;
      if (grp == 0 && ks > 0 && ks + 2 < 8) { WAIT_LGKM0(); issue_wo(ks + 2); }
      if (grp == 1 && ks < 7) {
        if (ks < 6) wait_vm<4>(); else wait_vm<0>();
        loada(af[(ks + 1) & 1], ks + 1);
      }
      if (idx + 1 < 24) loadb(bo[(idx + 1) & 1], idx + 1);
      SB();
#pragma unroll
      for (int j = 0; j < 3; ++j)
#pragma unroll
        for (int i = 0; i < 4; ++i) facc[i][grp * 3 + j] = MFMA(af[ks & 1][i], bo[idx & 1][j], facc[i][grp * 3 + j]);
      SB();
    }
    char* pp = (char*)partial + (size_t)((p * NB + b) * NH + h) * (126 * 1024) + lane * 16;
#pragma unroll
    for (int i = 0; i < 2; ++i)
#pragma unroll
      for (int n = 0; n < NT; ++n) {
        u32x4 v = {cvtpk(facc[2 * i][n][0], facc[2 * i][n][1]), cvtpk(facc[2 * i][n][2], facc[2 * i][n][3]),
                   cvtpk(facc[2 * i + 1][n][0], facc[2 * i + 1][n][1]), cvtpk(facc[2 * i + 1][n][2], facc[2 * i + 1][n][3])};
        *(u32x4*)(pp + (n * 14 + 2 * w + i) * 1024) = v;
      }
  }
}

__global__ __launch_bounds__(256) void reduce_heads(const float* __restrict__ partial, __hip_bfloat16* __restrict__ out) {
  const int idx = blockIdx.x * 256 + threadIdx.x;
  const int lane = idx & 63;
  const int T = (idx >> 6) % 126;
  const int pb = (idx >> 6) / 126;
  if (pb >= 16) return;
  const char* src = (const char*)partial + (size_t)pb * NH * (126 * 1024) + T * 1024 + lane * 16;
  u32x4 v[NH];
#pragma unroll
  for (int hh = 0; hh < NH; ++hh) v[hh] = *(const u32x4*)(src + (size_t)hh * (126 * 1024));
  float s[8] = {0.f, 0.f, 0.f, 0.f, 0.f, 0.f, 0.f, 0.f};
#pragma unroll
  for (int hh = 0; hh < NH; ++hh)
#pragma unroll
    for (int j = 0; j < 4; ++j) {
      s[2 * j] += __uint_as_float(v[hh][j] << 16);
      s[2 * j + 1] += __uint_as_float(v[hh][j] & 0xffff0000u);
    }
  const int n = T / 14, pr = T % 14;
  const int p = pb >> 3, b = pb & 7;
  const int row = 16 * n + (lane & 15), col = 32 * pr + 4 * (lane >> 4);
  char* o = (char*)out + ((size_t)(b * SQ + row) * (2 * DM) + p * DM + col) * 2;
  *(u32x2*)o = (u32x2){cvtpk(s[0], s[1]), cvtpk(s[2], s[3])};
  *(u32x2*)(o + 32) = (u32x2){cvtpk(s[4], s[5]), cvtpk(s[6], s[7])};
}

}

extern "C" void kernel_launch(void* const* d_in, const int* in_sizes, int n_in,
                              void* d_out, int out_size, void* d_ws, size_t ws_size,
                              hipStream_t stream) {
  (void)in_sizes; (void)n_in; (void)out_size; (void)ws_size;
  (void)hipFuncSetAttribute((const void*)fused_attn, hipFuncAttributeMaxDynamicSharedMemorySize, LDS_TOTAL);
  float* partial = (float*)d_ws;
  fused_attn<<<256, 512, LDS_TOTAL, stream>>>(
      (const __hip_bfloat16*)d_in[0], (const __hip_bfloat16*)d_in[1], (const __hip_bfloat16*)d_in[2],
      (const __hip_bfloat16*)d_in[3], (const __hip_bfloat16*)d_in[4],
      (const __hip_bfloat16*)d_in[5], (const __hip_bfloat16*)d_in[6], (const __hip_bfloat16*)d_in[7],
      (const __hip_bfloat16*)d_in[8], (const __hip_bfloat16*)d_in[9], partial);
  reduce_heads<<<(16 * 126 * 64 + 255) / 256, 256, 0, stream>>>(partial, (__hip_bfloat16*)d_out);
}
```

```cpp
#include <hip/hip_runtime.h>
#include <hip/hip_bf16.h>
#include <stdint.h>


namespace {

typedef __attribute__((ext_vector_type(8))) short bf16x8;
typedef __attribute__((ext_vector_type(4))) short s16x4;
typedef __attribute__((ext_vector_type(4))) float f32x4;
typedef __attribute__((ext_vector_type(2))) float f32x2;
typedef __attribute__((ext_vector_type(4))) unsigned u32x4;
typedef __attribute__((ext_vector_type(2))) unsigned u32x2;
typedef __attribute__((ext_vector_type(2))) __bf16 bf16x2_t;
typedef __attribute__((address_space(3))) s16x4* lds_tr_ptr;

constexpr int NB = 8, SQ = 144, DM = 448, NH = 16, SK = 512, DH = 256;
constexpr int NT = 9;

constexpr int LDS_A = 0;
constexpr int LDS_B = 73728;
constexpr int LDS_STAT = 155648;
constexpr int LDS_TOTAL = 163840;
constexpr int QSTAGE = 51200;
constexpr int QX = 18432;

__device__ __forceinline__ void dma16(const void* sbase, unsigned voff, unsigned lds_dst) {
  unsigned keep;
  asm volatile("s_mov_b32 %0, m0\n\ts_mov_b32 m0, %3\n\ts_nop 0\n\tglobal_load_lds_dwordx4 %1, %2\n\ts_mov_b32 m0, %0"
               : "=&s"(keep) : "v"(voff), "s"(sbase), "s"(lds_dst) : "memory");
}
__device__ __forceinline__ bf16x8 gload16(const void* sbase, unsigned voff) {
  bf16x8 r;
  asm volatile("global_load_dwordx4 %0, %1, %2" : "=v"(r) : "v"(voff), "s"(sbase) : "memory");
  return r;
}
#define PIN4(a) asm volatile("" : "+v"(a[0]), "+v"(a[1]), "+v"(a[2]), "+v"(a[3]))
template <int N> __device__ __forceinline__ void wait_vm() { asm volatile("s_waitcnt vmcnt(%0)" ::"i"(N) : "memory"); }
#define WAIT_LGKM0() asm volatile("s_waitcnt lgkmcnt(0)" ::: "memory")
#define SB() __builtin_amdgcn_sched_barrier(0)
__device__ __forceinline__ void bar() { WAIT_LGKM0(); __builtin_amdgcn_s_barrier(); asm volatile("" ::: "memory"); }

__device__ __forceinline__ unsigned cvtpk(float lo, float hi) {
  f32x2 v = {lo, hi};
  bf16x2_t r = __builtin_convertvector(v, bf16x2_t);
  return __builtin_bit_cast(unsigned, r);
}
__device__ __forceinline__ bf16x8 tr2(const char* base, unsigned a0, unsigned a1) {
  s16x4 l = __builtin_amdgcn_ds_read_tr16_b64_v4i16((lds_tr_ptr)(base + a0));
  s16x4 h = __builtin_amdgcn_ds_read_tr16_b64_v4i16((lds_tr_ptr)(base + a1));
  return (bf16x8){l[0], l[1], l[2], l[3], h[0], h[1], h[2], h[3]};
}
__device__ __forceinline__ float xmax(float v) {
  auto a = __builtin_amdgcn_permlane16_swap(__float_as_uint(v), __float_as_uint(v), false, false);
  v = fmaxf(__uint_as_float(a[0]), __uint_as_float(a[1]));
  auto b = __builtin_amdgcn_permlane32_swap(__float_as_uint(v), __float_as_uint(v), false, false);
  return fmaxf(__uint_as_float(b[0]), __uint_as_float(b[1]));
}
__device__ __forceinline__ float xsum(float v) {
  auto a = __builtin_amdgcn_permlane16_swap(__float_as_uint(v), __float_as_uint(v), false, false);
  v = __uint_as_float(a[0]) + __uint_as_float(a[1]);
  auto b = __builtin_amdgcn_permlane32_swap(__float_as_uint(v), __float_as_uint(v), false, false);
  return __uint_as_float(b[0]) + __uint_as_float(b[1]);
}
template <int NM, int NR> __device__ __forceinline__ void ilv() {
#pragma unroll
  for (int i = 0; i < NM; ++i) {
    __builtin_amdgcn_sched_group_barrier(0x008, 1, 0);
    if (i < NR) __builtin_amdgcn_sched_group_barrier(0x100, 1, 0);
  }
}
#define MFMA(a, b, c) __builtin_amdgcn_mfma_f32_16x16x32_bf16(a, b, c, 0, 0, 0)

template <int NA, int NN> struct Frag { bf16x8 a[NA]; bf16x8 b[NN]; };
template <int NA, int NN>
__device__ __forceinline__ void mma(f32x4 (&acc)[NA][NN], const Frag<NA, NN>& f) {
#pragma unroll
  for (int n = 0; n < NN; ++n)
#pragma unroll
    for (int m = 0; m < NA; ++m) acc[m][n] = MFMA(f.a[m], f.b[n], acc[m][n]);
}

#define RELANE() int lane = threadIdx.x & 63; asm volatile("" : "+v"(lane)); \
  const int l15 = lane & 15, g = lane >> 4, q4 = (lane >> 2) & 3, p4 = lane & 3; (void)l15; (void)g; (void)q4; (void)p4;
struct Ctx {
  char* smem;
  int lane, w, l15, g, q4, p4, hi;
  const char *xb, *kh, *vh, *wqh, *woh;
};

template <int NN, int XCNT, class KPRE>
__device__ __forceinline__ void phase_q(const Ctx& c, int nb, KPRE kpre) {
  char* smem = c.smem;
  const int w = c.w, lane = c.lane, g = c.g, l15 = c.l15, q4 = c.q4, p4 = c.p4, hi = c.hi, cc = c.w & 3;
  const unsigned x_voff = (unsigned)((8 * w + (lane >> 3)) * 896 + (((lane & 7) ^ ((((w & 1) << 2) + g) & 7)) << 4));
  unsigned wq_voff[4];
#pragma unroll
  for (int jj = 0; jj < 4; ++jj) {
    const int r = 2 * jj + hi;
    const int f = (r & 3) | ((w & 1) << 2);
    wq_voff[jj] = (unsigned)((8 * w + r) * 8192 + (((lane & 31) ^ (f << 1)) << 4));
  }
  auto issue_piece = [&](int s, int i) {
    const unsigned dst = (s % 3) * QSTAGE;
    if (i < 4) dma16(c.wqh + (size_t)s * 64 * 8192, wq_voff[i], dst + QX + (4 * w + i) * 1024);
    else dma16(c.xb + s * 128 + (i - 4) * 64 * 896, x_voff, dst + (w + 8 * (i - 4)) * 1024);
  };
  auto issue = [&](int s) {
#pragma unroll
    for (int i = 0; i < XCNT + 4; ++i) issue_piece(s, i);
  };
  constexpr int CNT = XCNT + 4;
  const int ftr = q4 | ((g & 1) << 2);
  unsigned qa[4];
#pragma unroll
  for (int m = 0; m < 4; ++m) qa[m] = QX + (8 * g + q4) * 512 + (((4 * cc + m) ^ ftr) << 5) + p4 * 8;
  const unsigned xr0 = (nb * 16 + l15) * 128 + ((g ^ (l15 >> 1)) << 4);
  const unsigned xr1 = xr0 ^ 64;
  auto load = [&](Frag<4, NN>& f, int s, int ks) {
    const char* sb = smem + (s % 3) * QSTAGE;
#pragma unroll
    for (int m = 0; m < 4; ++m) f.a[m] = tr2(sb, qa[m] + ks * 16384, qa[m] + ks * 16384 + 2048);
#pragma unroll
    for (int n = 0; n < NN; ++n) f.b[n] = *(const bf16x8*)(sb + (ks ? xr1 : xr0) + n * 2048);
  };
  f32x4 acc[4][NN];
#pragma unroll
  for (int m = 0; m < 4; ++m)
#pragma unroll
    for (int n = 0; n < NN; ++n) acc[m][n] = (f32x4){0.f, 0.f, 0.f, 0.f};
  Frag<4, NN> f0, f1;
  issue(0); issue(1); issue(2); wait_vm<2 * CNT>();
  bar();
  load(f0, 0, 0);
#pragma unroll
  for (int s = 0; s < 7; ++s) {
    load(f1, s, 1);
    mma<4, NN>(acc, f0);
    ilv<4 * NN, 8 + NN>();
    SB();
    if (s < 6) {
      if (XCNT) { if (s < 4) wait_vm<CNT>(); else if (s == 4) wait_vm<CNT + 12>(); else wait_vm<12>(); }
      bar();
      load(f0, s + 1, 0);
    }
#pragma unroll
    for (int n = 0; n < NN; ++n) {
#pragma unroll
      for (int m = 0; m < 4; ++m) acc[m][n] = MFMA(f1.a[m], f1.b[n], acc[m][n]);
      SB();
      if (s + 3 < 7) {
        if (2 * n < CNT) issue_piece(s + 3, 2 * n);
        if (2 * n + 1 < CNT) issue_piece(s + 3, 2 * n + 1);
      }
      SB();
    }
    if (s == 3) kpre();
    SB();
  }
  bar();
#pragma unroll
  for (int m = 0; m < 4; ++m)
#pragma unroll
    for (int n = 0; n < NN; ++n) {
      u32x2 v = {cvtpk(acc[m][n][0], acc[m][n][1]), cvtpk(acc[m][n][2], acc[m][n][3])};
      const unsigned a = (16 * (nb + n) + l15) * 512 + (((8 * cc + 2 * m + (g >> 1)) ^ l15) << 4) + (g & 1) * 8;
      *(u32x2*)(smem + LDS_A + a) = v;
    }
}

__global__ __launch_bounds__(512, 2) void fused_attn(
    const __hip_bfloat16* __restrict__ xA, const __hip_bfloat16* __restrict__ kA, const __hip_bfloat16* __restrict__ vA,
    const __hip_bfloat16* __restrict__ wqA, const __hip_bfloat16* __restrict__ woA,
    const __hip_bfloat16* __restrict__ xB, const __hip_bfloat16* __restrict__ kB, const __hip_bfloat16* __restrict__ vB,
    const __hip_bfloat16* __restrict__ wqB, const __hip_bfloat16* __restrict__ woB,
    float* __restrict__ partial) {
  extern __shared__ __attribute__((aligned(16))) char smem[];
  const int tid = threadIdx.x;
  Ctx c;
  c.smem = smem;
  c.lane = tid & 63;
  c.w = __builtin_amdgcn_readfirstlane(tid >> 6);
  c.l15 = c.lane & 15; c.g = c.lane >> 4; c.q4 = (c.lane >> 2) & 3; c.p4 = c.lane & 3; c.hi = c.lane >> 5;
  const int w = c.w;

  const int bid = blockIdx.x;
  const int xcd = bid & 7, rr = bid >> 3;
  const int p = xcd >> 2, h = (xcd & 3) * 4 + (rr >> 3), b = rr & 7;

  c.xb = (const char*)((p ? xB : xA) + (size_t)b * SQ * DM);
  c.kh = (const char*)((p ? kB : kA) + (size_t)(b * NH + h) * SK * DH);
  c.vh = (const char*)((p ? vB : vA) + (size_t)(b * NH + h) * SK * DH);
  c.wqh = (const char*)((p ? wqB : wqA) + h * DH);
  c.woh = (const char*)((p ? woB : woA) + (size_t)h * DH * DM);

  unsigned v_voff, k_voff;
  { RELANE(); v_voff = (unsigned)((lane >> 2) * 512 + 64 * w + (((lane & 3) ^ ((g & 1) << 1)) << 4));
    k_voff = (unsigned)((64 * w + l15) * 512 + g * 16); }
  const unsigned vring = LDS_B + w * 2048;
  auto vsrc = [&](int t) { return c.vh + (size_t)(64 * (t & 7) + 32 * (t >> 3)) * 512; };
  auto issue_v = [&](int t, int slot) {
    dma16(vsrc(t), v_voff, vring + slot * 16384);
    dma16(vsrc(t) + 16 * 512, v_voff, vring + slot * 16384 + 1024);
  };
  bf16x8 kf[3][4];
  auto load_k = [&](int ks) {
#pragma unroll
    for (int m = 0; m < 4; ++m) kf[ks % 3][m] = gload16(c.kh + m * 16 * 512 + ks * 64, k_voff);
  };
  auto kpre = [&]() { load_k(0); load_k(1); load_k(2); };
  if (w < 2) phase_q<5, 3>(c, 0, kpre); else if (w < 4) phase_q<5, 2>(c, 0, kpre); else phase_q<4, 2>(c, 5, kpre);
  bar();
  f32x4 sacc[4][NT];
#pragma unroll
  for (int m = 0; m < 4; ++m)
#pragma unroll
    for (int n = 0; n < NT; ++n) sacc[m][n] = (f32x4){0.f, 0.f, 0.f, 0.f};
  {
    RELANE();
    const unsigned rowb = LDS_A + l15 * 512;
    bf16x8 bq[2][3];
    auto loadq = [&](bf16x8 (&d)[3], int idx) {
      const int ks = idx / 3, grp = idx % 3;
      const unsigned ca = rowb + (((4 * ks + g) ^ l15) << 4);
#pragma unroll
      for (int j = 0; j < 3; ++j) d[j] = *(const bf16x8*)(smem + ca + (grp * 3 + j) * 8192);
    };
    loadq(bq[0], 0);
#pragma unroll
    for (int idx = 0; idx < 24; ++idx) {
      const int ks = idx / 3, grp = idx % 3;
      if (idx + 1 < 24) loadq(bq[(idx + 1) & 1], idx + 1);
      if (grp == 0) {
        if (ks < 5) wait_vm<8>(); else if (ks == 5) wait_vm<18>(); else if (ks == 6) wait_vm<14>(); else wait_vm<10>();
        PIN4(kf[ks % 3]);
      }
      SB();
#pragma unroll
      for (int j = 0; j < 3; ++j)
#pragma unroll
        for (int m = 0; m < 4; ++m) sacc[m][grp * 3 + j] = MFMA(kf[ks % 3][m], bq[idx & 1][j], sacc[m][grp * 3 + j]);
      if (grp == 2 && ks < 5) load_k(ks + 3);
      if (grp == 2 && ks == 4) { issue_v(0, 0); issue_v(1, 1); issue_v(2, 2); issue_v(3, 3); issue_v(4, 4); }
    }
  }
  bf16x8 vr[8][2];
#pragma unroll
  for (int t = 0; t < 8; ++t) { vr[t][0] = gload16(vsrc(t + 5), v_voff); vr[t][1] = gload16(vsrc(t + 5) + 16 * 512, v_voff); }
  float* smax = (float*)(smem + LDS_STAT);
  float* ssum = (float*)(smem + LDS_STAT);
  u32x4 pk1[NT];
  {
  RELANE();
#pragma unroll
  for (int n = 0; n < NT; ++n) {
    float m0 = fmaxf(fmaxf(sacc[0][n][0], sacc[0][n][1]), fmaxf(sacc[0][n][2], sacc[0][n][3]));
    float m1 = fmaxf(fmaxf(sacc[1][n][0], sacc[1][n][1]), fmaxf(sacc[1][n][2], sacc[1][n][3]));
    float m2 = fmaxf(fmaxf(sacc[2][n][0], sacc[2][n][1]), fmaxf(sacc[2][n][2], sacc[2][n][3]));
    float m3 = fmaxf(fmaxf(sacc[3][n][0], sacc[3][n][1]), fmaxf(sacc[3][n][2], sacc[3][n][3]));
    const float m = xmax(fmaxf(fmaxf(m0, m1), fmaxf(m2, m3)));
    smax[(16 * n + l15) * 8 + w] = m;
  }
  bar();
  constexpr float CEXP = 0.0625f * 1.4426950408889634f;
  float mc[NT];
#pragma unroll
  for (int n = 0; n < NT; ++n) {
    const f32x4 m0 = *(const f32x4*)(smax + (16 * n + l15) * 8);
    const f32x4 m1 = *(const f32x4*)(smax + (16 * n + l15) * 8 + 4);
    mc[n] = -CEXP * fmaxf(fmaxf(fmaxf(m0[0], m0[1]), fmaxf(m0[2], m0[3])), fmaxf(fmaxf(m1[0], m1[1]), fmaxf(m1[2], m1[3])));
  }
  float rsum[NT];
  const unsigned pw = LDS_A + l15 * 512 + (((4 * w + g) ^ l15) << 4);
#pragma unroll
  for (int n = 0; n < NT; ++n) {
    float e[4][4];
#pragma unroll
    for (int mm = 0; mm < 4; ++mm)
#pragma unroll
      for (int r = 0; r < 4; ++r) e[mm][r] = __builtin_amdgcn_exp2f(fmaf(sacc[mm][n][r], CEXP, mc[n]));
    const float s0 = (e[0][0] + e[0][1]) + (e[0][2] + e[0][3]), s1 = (e[1][0] + e[1][1]) + (e[1][2] + e[1][3]);
    const float s2 = (e[2][0] + e[2][1]) + (e[2][2] + e[2][3]), s3 = (e[3][0] + e[3][1]) + (e[3][2] + e[3][3]);
    rsum[n] = xsum((s0 + s1) + (s2 + s3));
    u32x4 p0 = {cvtpk(e[0][0], e[0][1]), cvtpk(e[0][2], e[0][3]), cvtpk(e[1][0], e[1][1]), cvtpk(e[1][2], e[1][3])};
    *(u32x4*)(smem + pw + n * 8192) = p0;
    pk1[n] = (u32x4){cvtpk(e[2][0], e[2][1]), cvtpk(e[2][2], e[2][3]), cvtpk(e[3][0], e[3][1]), cvtpk(e[3][2], e[3][3])};
  }
  bar();
#pragma unroll
  for (int n = 0; n < NT; ++n) ssum[(16 * n + l15) * 8 + w] = rsum[n];
  }
  f32x4 oacc[2][NT];
#pragma unroll
  for (int m = 0; m < 2; ++m)
#pragma unroll
    for (int n = 0; n < NT; ++n) oacc[m][n] = (f32x4){0.f, 0.f, 0.f, 0.f};
  {
    RELANE();
    const unsigned pw = LDS_A + l15 * 512 + (((4 * w + g) ^ l15) << 4);
    const unsigned va0 = vring + (4 * g + q4) * 64 + ((0 ^ (g & 1)) << 5) + p4 * 8;
    const unsigned va1 = vring + (4 * g + q4) * 64 + ((1 ^ (g & 1)) << 5) + p4 * 8;
    const unsigned rowb = LDS_A + l15 * 512;
    auto slot_of = [](int t) { return t < 5 ? t : (t < 13 ? 3 + ((t - 5) & 1) : t - 13); };
    bf16x8 af[2][2], bp[2][3];
    auto loada = [&](bf16x8 (&d)[2], int t) {
      const char* vs = smem + slot_of(t) * 16384;
      d[0] = tr2(vs, va0, va0 + 1024);
      d[1] = tr2(vs, va1, va1 + 1024);
    };
    auto loadb = [&](bf16x8 (&d)[3], int idx) {
      const int t = idx / 3, grp = idx % 3;
      const unsigned ca = rowb + (((4 * (t & 7) + g) ^ l15) << 4);
#pragma unroll
      for (int j = 0; j < 3; ++j) d[j] = *(const bf16x8*)(smem + ca + (grp * 3 + j) * 8192);
    };
    wait_vm<24>();
    loada(af[0], 0);
    loadb(bp[0], 0);
#pragma unroll
    for (int idx = 0; idx < 48; ++idx) {
      const int t = idx / 3, grp = idx % 3;
      if (grp == 0 && t < 15) {
        if (t >= 1 && t <= 3) { WAIT_LGKM0(); issue_v(t + 12, t - 1); }
        if (t <= 3) wait_vm<22>(); else if (t == 4) wait_vm<20>(); else if (t == 5) wait_vm<18>(); else if (t == 6) wait_vm<16>();
        else if (t == 7) wait_vm<14>(); else if (t == 8) wait_vm<12>(); else if (t == 9) wait_vm<10>(); else if (t == 10) wait_vm<8>();
        else if (t == 11) wait_vm<6>(); else if (t == 12) wait_vm<4>(); else if (t == 13) wait_vm<2>(); else wait_vm<0>();
        if (t + 1 >= 5 && t + 1 < 13) {
          asm volatile("" : "+v"(vr[t - 4][0]), "+v"(vr[t - 4][1]));
          char* dst = smem + vring + slot_of(t + 1) * 16384 + lane * 16;
          *(bf16x8*)dst = vr[t - 4][0];
          *(bf16x8*)(dst + 1024) = vr[t - 4][1];
        }
        loada(af[(t + 1) & 1], t + 1);
      }
      if (idx == 23) {
        bar();
#pragma unroll
        for (int n = 0; n < NT; ++n) *(u32x4*)(smem + pw + n * 8192) = pk1[n];
        bar();
      }
      if (idx + 1 < 48) loadb(bp[(idx + 1) & 1], idx + 1);
      SB();
#pragma unroll
      for (int j = 0; j < 3; ++j) {
        oacc[0][grp * 3 + j] = MFMA(af[t & 1][0], bp[idx & 1][j], oacc[0][grp * 3 + j]);
        oacc[1][grp * 3 + j] = MFMA(af[t & 1][1], bp[idx & 1][j], oacc[1][grp * 3 + j]);
      }
      SB();
    }
  }
  RELANE();
  const unsigned pw = LDS_A + l15 * 512 + (((4 * w + g) ^ l15) << 4);
  float rinv[NT];
#pragma unroll
  for (int n = 0; n < NT; ++n) {
    const f32x4 s0 = *(const f32x4*)(ssum + (16 * n + l15) * 8);
    const f32x4 s1 = *(const f32x4*)(ssum + (16 * n + l15) * 8 + 4);
    rinv[n] = 1.0f / (((s0[0] + s0[1]) + (s0[2] + s0[3])) + ((s1[0] + s1[1]) + (s1[2] + s1[3])));
  }
  bar();

  const unsigned wo_voff = (unsigned)((lane >> 3) * 896 + 128 * w + (((lane & 7) ^ (g << 1)) << 4));
  const unsigned woring = LDS_B + w * 12288;
  auto issue_wo = [&](int ks) {
    const char* src = c.woh + (size_t)ks * 32 * 896;
    const unsigned dst = woring + (ks % 3) * 4096;
#pragma unroll
    for (int j = 0; j < 4; ++j) dma16(src + j * 8 * 896, wo_voff, dst + j * 1024);
  };
  if (w < 7) { issue_wo(0); issue_wo(1); issue_wo(2); }
#pragma unroll
  for (int n = 0; n < NT; ++n) {
    const float s = rinv[n];
    u32x4 v = {cvtpk(oacc[0][n][0] * s, oacc[0][n][1] * s), cvtpk(oacc[0][n][2] * s, oacc[0][n][3] * s),
               cvtpk(oacc[1][n][0] * s, oacc[1][n][1] * s), cvtpk(oacc[1][n][2] * s, oacc[1][n][3] * s)};
    *(u32x4*)(smem + pw + n * 8192) = v;
  }
  bar();
  if (w < 7) {
    const int swo = (2 * g + (q4 >> 1)) & 3;
    unsigned oa[4];
#pragma unroll
    for (int i = 0; i < 4; ++i) oa[i] = woring + (4 * g + q4) * 128 + ((i ^ swo) << 5) + p4 * 8;
    const unsigned rowb = LDS_A + l15 * 512;
    f32x4 facc[4][NT];
#pragma unroll
    for (int i = 0; i < 4; ++i)
#pragma unroll
      for (int n = 0; n < NT; ++n) facc[i][n] = (f32x4){0.f, 0.f, 0.f, 0.f};
    bf16x8 af[2][4], bo[2][3];
    auto loada = [&](bf16x8 (&d)[4], int ks) {
#pragma unroll
      for (int i = 0; i < 4; ++i) d[i] = tr2(smem + (ks % 3) * 4096, oa[i], oa[i] + 2048);
    };
    auto loadb = [&](bf16x8 (&d)[3], int idx) {
      const int ks = idx / 3, grp = idx % 3;
      const unsigned ca = rowb + (((4 * ks + g) ^ l15) << 4);
#pragma unroll
      for (int j = 0; j < 3; ++j) d[j] = *(const bf16x8*)(smem + ca + (grp * 3 + j) * 8192);
    };
    wait_vm<8>();
    loada(af[0], 0);
    loadb(bo[0], 0);
#pragma unroll
    for (int idx = 0; idx < 24; ++idx) {
      const int ks = idx / 3, grp = idx % 3;
      if (grp == 0 && ks > 0 && ks + 2 < 8) { WAIT_LGKM0(); issue_wo(ks + 2); }
      if (grp == 1 && ks < 7) {
        if (ks < 6) wait_vm<4>(); else wait_vm<0>();
        loada(af[(ks + 1) & 1], ks + 1);
      }
      if (idx + 1 < 24) loadb(bo[(idx + 1) & 1], idx + 1);
      SB();
#pragma unroll
      for (int j = 0; j < 3; ++j)
#pragma unroll
        for (int i = 0; i < 4; ++i) facc[i][grp * 3 + j] = MFMA(af[ks & 1][i], bo[idx & 1][j], facc[i][grp * 3 + j]);
      SB();
    }
    char* pp = (char*)partial + (size_t)((p * NB + b) * NH + h) * (126 * 1024) + lane * 16;
#pragma unroll
    for (int i = 0; i < 2; ++i)
#pragma unroll
      for (int n = 0; n < NT; ++n) {
        u32x4 v = {cvtpk(facc[2 * i][n][0], facc[2 * i][n][1]), cvtpk(facc[2 * i][n][2], facc[2 * i][n][3]),
                   cvtpk(facc[2 * i + 1][n][0], facc[2 * i + 1][n][1]), cvtpk(facc[2 * i + 1][n][2], facc[2 * i + 1][n][3])};
        *(u32x4*)(pp + (n * 14 + 2 * w + i) * 1024) = v;
      }
  }
}

__global__ __launch_bounds__(256) void reduce_heads(const float* __restrict__ partial, __hip_bfloat16* __restrict__ out) {
  const int idx = blockIdx.x * 256 + threadIdx.x;
  const int lane = idx & 63;
  const int T = (idx >> 6) % 126;
  const int pb = (idx >> 6) / 126;
  if (pb >= 16) return;
  const char* src = (const char*)partial + (size_t)pb * NH * (126 * 1024) + T * 1024 + lane * 16;
  u32x4 v[NH];
#pragma unroll
  for (int hh = 0; hh < NH; ++hh) v[hh] = *(const u32x4*)(src + (size_t)hh * (126 * 1024));
  float s[8] = {0.f, 0.f, 0.f, 0.f, 0.f, 0.f, 0.f, 0.f};
#pragma unroll
  for (int hh = 0; hh < NH; ++hh)
#pragma unroll
    for (int j = 0; j < 4; ++j) {
      s[2 * j] += __uint_as_float(v[hh][j] << 16);
      s[2 * j + 1] += __uint_as_float(v[hh][j] & 0xffff0000u);
    }
  const int n = T / 14, pr = T % 14;
  const int p = pb >> 3, b = pb & 7;
  const int row = 16 * n + (lane & 15), col = 32 * pr + 4 * (lane >> 4);
  char* o = (char*)out + ((size_t)(b * SQ + row) * (2 * DM) + p * DM + col) * 2;
  *(u32x2*)o = (u32x2){cvtpk(s[0], s[1]), cvtpk(s[2], s[3])};
  *(u32x2*)(o + 32) = (u32x2){cvtpk(s[4], s[5]), cvtpk(s[6], s[7])};
}

}

extern "C" void kernel_launch(void* const* d_in, const int* in_sizes, int n_in,
                              void* d_out, int out_size, void* d_ws, size_t ws_size,
                              hipStream_t stream) {
  (void)in_sizes; (void)n_in; (void)out_size; (void)ws_size;
  (void)hipFuncSetAttribute((const void*)fused_attn, hipFuncAttributeMaxDynamicSharedMemorySize, LDS_TOTAL);
  float* partial = (float*)d_ws;
  fused_attn<<<256, 512, LDS_TOTAL, stream>>>(
      (const __hip_bfloat16*)d_in[0], (const __hip_bfloat16*)d_in[1], (const __hip_bfloat16*)d_in[2],
      (const __hip_bfloat16*)d_in[3], (const __hip_bfloat16*)d_in[4],
      (const __hip_bfloat16*)d_in[5], (const __hip_bfloat16*)d_in[6], (const __hip_bfloat16*)d_in[7],
      (const __hip_bfloat16*)d_in[8], (const __hip_bfloat16*)d_in[9], partial);
  reduce_heads<<<(16 * 126 * 64 + 255) / 256, 256, 0, stream>>>(partial, (__hip_bfloat16*)d_out);
}
```
